```python
import torch

def _softmax(x, axis=-1):
    return torch.softmax(x, dim=axis)
def _gelu(x):
    return torch.nn.functional.gelu(x, approximate="tanh")
def _silu(x):
    return torch.nn.functional.silu(x)
def _sigmoid(x):
    return torch.sigmoid(x)
def _layernorm(x, axis=-1, eps=1e-5):
    m = x.mean(dim=axis, keepdim=True)
    v = x.var(dim=axis, keepdim=True, unbiased=False)
    return (x - m) / torch.sqrt(v + eps)
def _scatter_add(tgt, idx, src, axis=0):
    return tgt.clone().index_add_(axis, idx, src)
def _segment_sum(src, seg, n):
    out = torch.zeros((n,) + src.shape[1:], dtype=src.dtype)
    return out.index_add_(0, seg, src)
def _pad(x, pw):
    flat = tuple(v for pair in reversed(pw) for v in pair)
    return torch.nn.functional.pad(x, flat)

kernel_name = 'concat_heads__linear_attn_block__linear_attn_block__prompt'

def setup_inputs():

    _w = (lambda a: (lambda t: t.to(torch.bfloat16) if t.is_floating_point() else t)(torch.as_tensor(a)))
    def _A_setup():
        B, S, D = 1, 2048, 512
        return {
            "x": _w((lambda *s: torch.randn(*s, dtype=torch.bfloat16) * 0.05)(B, S, D)),
            "wqkv": _w((lambda *s: torch.randn(*s, dtype=torch.bfloat16) * 0.4)(D, 3 * D)),
            "wo": _w((lambda *s: torch.randn(*s, dtype=torch.bfloat16) * 0.05)(D, D)),
        }
    _A_in = _A_setup()

    def _B_setup():
        B, S, D = 1, 2048, 512
        return {
            "x": _w((lambda *s: torch.randn(*s, dtype=torch.bfloat16) * 0.05)(B, S, D)),
            "wqkv": _w((lambda *s: torch.randn(*s, dtype=torch.bfloat16) * 0.4)(D, 3 * D)),
            "wo": _w((lambda *s: torch.randn(*s, dtype=torch.bfloat16) * 0.05)(D, D)),
        }
    _B_in = _B_setup()

    return {
        "A_x": _A_in["x"],
        "A_wqkv": _A_in["wqkv"],
        "A_wo": _A_in["wo"],
        "B_x": _B_in["x"],
        "B_wqkv": _B_in["wqkv"],
        "B_wo": _B_in["wo"],
    }

def reference(A_x, A_wqkv, A_wo, B_x, B_wqkv, B_wo):
    def _A_ref(x, wqkv, wo):
        B, S, D = x.shape
        H = 32; Dh = D // H
        qkv = (lambda x, *d: x.permute(*d))((x @ wqkv).reshape(B, S, 3, H, Dh), 2, 0, 3, 1, 4)
        q, k, v = qkv[0], qkv[1], qkv[2]
        phi = lambda t: torch.where(t > 0, t + 1, torch.exp(t)) + 1e-6
        pq, pk = phi(q), phi(k)
        kv = torch.einsum("bhsd,bhse->bhde", pk, v)
        z = torch.einsum("bhsd->bhd", pk)
        num = torch.einsum("bhqd,bhde->bhqe", pq, kv)
        den = torch.einsum("bhqd,bhd->bhq", pq, z)[..., None] + 1e-6
        o = num / den
        return (lambda x, *d: x.permute(*d))(o, 0, 2, 1, 3).reshape(B, S, D) @ wo

    def _B_ref(x, wqkv, wo):
        B, S, D = x.shape
        H = 32; Dh = D // H
        qkv = (lambda x, *d: x.permute(*d))((x @ wqkv).reshape(B, S, 3, H, Dh), 2, 0, 3, 1, 4)
        q, k, v = qkv[0], qkv[1], qkv[2]
        phi = lambda t: torch.where(t > 0, t + 1, torch.exp(t)) + 1e-6
        pq, pk = phi(q), phi(k)
        kv = torch.einsum("bhsd,bhse->bhde", pk, v)
        z = torch.einsum("bhsd->bhd", pk)
        num = torch.einsum("bhqd,bhde->bhqe", pq, kv)
        den = torch.einsum("bhqd,bhd->bhq", pq, z)[..., None] + 1e-6
        o = num / den
        return (lambda x, *d: x.permute(*d))(o, 0, 2, 1, 3).reshape(B, S, D) @ wo

    a = _A_ref(A_x, A_wqkv, A_wo)
    b = _B_ref(B_x, B_wqkv, B_wo)
    return torch.concatenate([a, b], axis=-1)
```

```cpp
#include <hip/hip_runtime.h>
#include <hip/hip_bf16.h>
#include <stdint.h>

typedef unsigned short u16;
__device__ __forceinline__ float bf2f(u16 v){ return __uint_as_float(((unsigned)v)<<16); }
__device__ __forceinline__ u16 f2bf(float f){ __hip_bfloat16 b = __float2bfloat16(f); return *(u16*)&b; }
__device__ __forceinline__ float rbf(float f){ return bf2f(f2bf(f)); }
__device__ __forceinline__ float phi(float t){ float r = t>0.f ? rbf(t+1.f) : rbf(__expf(t)); return rbf(r+1e-6f); }

__global__ void k1(const u16* x, const u16* w, u16* qkv){
  int n = blockIdx.x*256+threadIdx.x; int s = blockIdx.y;
  float acc=0.f;
  for(int k=0;k<512;++k) acc += bf2f(x[s*512+k])*bf2f(w[k*1536+n]);
  float t = rbf(acc);
  if(n<1024) t = phi(t);
  qkv[s*1536+n]=f2bf(t);
}
__global__ void k2(const u16* qkv, float* kv, float* z){
  int h = blockIdx.x; int d = threadIdx.x>>4, e = threadIdx.x&15;
  float acc=0.f, zz=0.f;
  for(int s=0;s<2048;++s){ float pk=bf2f(qkv[s*1536+512+h*16+d]); float v=bf2f(qkv[s*1536+1024+h*16+e]); acc+=pk*v; zz+=pk; }
  kv[h*256+d*16+e]=rbf(acc);
  if(e==0) z[h*16+d]=rbf(zz);
}
__global__ void k3(const u16* qkv, const float* kv, const float* z, u16* o){
  int s = blockIdx.x; int c = threadIdx.x;
  int h=c>>4, e=c&15;
  float num=0.f, den=0.f;
  for(int d=0;d<16;++d){ float pq=bf2f(qkv[s*1536+h*16+d]); num+=pq*kv[h*256+d*16+e]; den+=pq*z[h*16+d]; }
  num=rbf(num); den=rbf(rbf(den)+1e-6f);
  o[s*512+c]=f2bf(num/den);
}
__global__ void k4(const u16* o, const u16* wo, u16* out, int coloff){
  int n = blockIdx.x*256+threadIdx.x; int s = blockIdx.y;
  float acc=0.f;
  for(int k=0;k<512;++k) acc += bf2f(o[s*512+k])*bf2f(wo[k*512+n]);
  out[s*1024+coloff+n]=f2bf(acc);
}

extern "C" void kernel_launch(void* const* d_in, const int* in_sizes, int n_in,
                              void* d_out, int out_size, void* d_ws, size_t ws_size,
                              hipStream_t stream) {
  char* ws=(char*)d_ws;
  for(int b=0;b<2;++b){
    const u16* x=(const u16*)d_in[3*b]; const u16* w=(const u16*)d_in[3*b+1]; const u16* wo=(const u16*)d_in[3*b+2];
    u16* qkv=(u16*)(ws + (size_t)b*(16<<20)); float* kv=(float*)(ws+(size_t)b*(16<<20)+(8<<20)); float* z=kv+32*256; u16* o=(u16*)(ws+(size_t)b*(16<<20)+(12<<20));
    k1<<<dim3(6,2048),256,0,stream>>>(x,w,qkv);
    k2<<<32,256,0,stream>>>(qkv,kv,z);
    k3<<<2048,512,0,stream>>>(qkv,kv,z,o);
    k4<<<dim3(2,2048),256,0,stream>>>(o,wo,(u16*)d_out,b*512);
  }
}
```

```cpp
#include <hip/hip_runtime.h>
#include <stdint.h>
#include <utility>

typedef unsigned u32;
typedef unsigned long long u64;
typedef u32 u32x4 __attribute__((ext_vector_type(4)));
typedef u32 u32x2 __attribute__((ext_vector_type(2)));
typedef float f32x4 __attribute__((ext_vector_type(4)));
typedef float f32x2 __attribute__((ext_vector_type(2)));
typedef short s16x4 __attribute__((ext_vector_type(4)));
typedef __bf16 bf16x8 __attribute__((ext_vector_type(8)));
typedef __bf16 bf16x2 __attribute__((ext_vector_type(2)));
#define AS3 __attribute__((address_space(3)))
#define AS1 __attribute__((address_space(1)))
#define AS4 __attribute__((address_space(4)))
#define DI __device__ __forceinline__
#define SB() __builtin_amdgcn_sched_barrier(0)
#define RLX_AGENT __ATOMIC_RELAXED, __HIP_MEMORY_SCOPE_AGENT

constexpr int SRD3 = 0x00020000;
constexpr u32 WR = 131072;
constexpr u32 LDS_BYTES = 163840;
constexpr u32 WS_EPOCH = 0;
constexpr u32 WS_F1 = 4096;
constexpr u32 WS_F2 = 8192;
constexpr u32 WS_PART = 65536;
constexpr u32 PART_BYTES = 4352;
constexpr u32 WS_O = 2u << 20;
constexpr u32 WS_USED = 6u << 20;
constexpr u32 SPIN_CAP = 1u << 16;

template <int... Is, class F> DI void sfor_impl(std::integer_sequence<int, Is...>, F&& f) { (f(std::integral_constant<int, Is>{}), ...); }
template <int N, class F> DI void sfor(F&& f) { sfor_impl(std::make_integer_sequence<int, N>{}, f); }

DI u32 rfl(u32 x) { return __builtin_amdgcn_readfirstlane(x); }
DI u32 cvtpk(float lo, float hi) { f32x2 v = {lo, hi}; bf16x2 b = __builtin_convertvector(v, bf16x2); return __builtin_bit_cast(u32, b); }
DI float lo16(u32 p) { return __uint_as_float(p << 16); }
DI float hi16(u32 p) { return __uint_as_float(p & 0xffff0000u); }
DI float phi1(float t) { return __builtin_fmaxf(t, 0.f) + __builtin_amdgcn_fmed3f(__builtin_amdgcn_exp2f(t * 1.44269504088896340736f), 0.f, 1.f); }
DI u32 phi2(float a, float b) {
  u32 t = cvtpk(a, b);
  u32 r = cvtpk(phi1(lo16(t)), phi1(hi16(t)));
  return cvtpk(lo16(r) + 1e-6f, hi16(r) + 1e-6f);
}
DI void dma(u32 ldsdst, u32 voff, __amdgpu_buffer_rsrc_t srd, u32 soff) {
  asm volatile("s_mov_b32 m0, %0\n\ts_nop 0\n\tbuffer_load_dwordx4 %1, %2, %3 offen lds" ::"s"(ldsdst), "v"(voff), "s"(srd), "s"(soff) : "m0", "memory");
}
template <int N> DI void wait_vm() { asm volatile("s_waitcnt vmcnt(%0)" ::"n"(N) : "memory"); }
template <int N> DI void wait_vm_bar() { asm volatile("s_waitcnt vmcnt(%0) lgkmcnt(0)\n\ts_barrier" ::"n"(N) : "memory"); }
DI void lgk_bar() { asm volatile("s_waitcnt lgkmcnt(0)\n\ts_barrier" ::: "memory"); }
DI u32x4 ldsr128(u32 a) { return *(const AS3 u32x4*)(a); }
DI u32x2 ldsr_tr(u32 a) { return __builtin_bit_cast(u32x2, __builtin_amdgcn_ds_read_tr16_b64_v4i16((AS3 s16x4*)(a))); }
DI bf16x8 mk8(u32x2 a, u32x2 b) { u32x4 v = {a[0], a[1], b[0], b[1]}; return __builtin_bit_cast(bf16x8, v); }
DI bf16x8 asb8(u32x4 v) { return __builtin_bit_cast(bf16x8, v); }
DI f32x4 mfma32(bf16x8 a, bf16x8 b, f32x4 c) { return __builtin_amdgcn_mfma_f32_16x16x32_bf16(a, b, c, 0, 0, 0); }
DI f32x4 mfma16(u32x2 a, u32x2 b, f32x4 c) { return __builtin_amdgcn_mfma_f32_16x16x16bf16_1k(__builtin_bit_cast(s16x4, a), __builtin_bit_cast(s16x4, b), c, 0, 0, 0); }

#define STAMP(i) do {} while (0)
#define CSTAMP(i) do {} while (0)

DI void poll_flags(const u64* flags, int n, u64 want, int lane) {
  const AS1 u64* p = (const AS1 u64*)flags + (lane < n ? lane : 0);
#pragma unroll 1
  for (u32 spins = 0; spins < SPIN_CAP; ++spins) {
    u64 v = __hip_atomic_load(p, RLX_AGENT);
    if (__all(v == want)) break;
    __builtin_amdgcn_s_sleep(1);
  }
  asm volatile("" ::: "memory");
}

__global__ __launch_bounds__(512, 2) void fused_linattn_kernel(const void* __restrict__ xA, const void* __restrict__ wA, const void* __restrict__ oA,
                                                               const void* __restrict__ xB, const void* __restrict__ wB, const void* __restrict__ oB,
                                                               void* __restrict__ outp, char* __restrict__ ws
) {
  extern __shared__ __attribute__((aligned(16))) char sm[];
  const u32 lds0 = (u32)(uintptr_t)(AS3 char*)sm;
  const int lane = threadIdx.x & 63;
  const u32 w = rfl(threadIdx.x >> 6);
  const u32 bid = blockIdx.x, xcd = bid & 7, j = bid >> 3;
  const u32 br = xcd & 1, r = ((xcd >> 1) & 1) * 8 + (j & 7), g = (xcd >> 2) * 4 + (j >> 3);
#define ROT (((j & 7) + 2 * (j >> 3)) & 7)
  const u32 rot = ROT;
  STAMP(0);
  const __amdgpu_buffer_rsrc_t rsX = __builtin_amdgcn_make_buffer_rsrc((void*)(br ? xB : xA), 0, 2048 * 512 * 2, SRD3);
  const __amdgpu_buffer_rsrc_t rsW = __builtin_amdgcn_make_buffer_rsrc((void*)(br ? wB : wA), 0, 512 * 1536 * 2, SRD3);
  const __amdgpu_buffer_rsrc_t rsO = __builtin_amdgcn_make_buffer_rsrc((void*)(br ? oB : oA), 0, 512 * 512 * 2, SRD3);
  const __amdgpu_buffer_rsrc_t rsS = __builtin_amdgcn_make_buffer_rsrc((void*)ws, 0, WS_USED, SRD3);
  const u32 G = lane >> 4, l15 = lane & 15, q4 = (lane >> 2) & 3, p4 = lane & 3;

  const u32 voffX = (r * 128 + 8 * w + (lane >> 3)) * 1024 + (((lane & 7) ^ (4 * (w & 1) + G)) * 16);
  u32 voffW;
  { u32 f = G | (((w >> 1) & 1) << 2), cb = (l15 >> 1) ^ f;
    voffW = (4 * w + G) * 3072 + (512 + g * 64 + (cb & 3) * 16 + (cb >> 2) * 512) * 2 + (lane & 1) * 16; }
  u32 voffQ;
  { u32 f = (G & 1) | ((w & 1) << 1), cb = ((lane & 7) >> 1) ^ f;
    voffQ = (8 * w + (lane >> 3)) * 3072 + (g * 64 + cb * 16) * 2 + (lane & 1) * 16; }
  u32 voffO;
  { u32 f = 4 * (w & 1) + G, cb = (l15 >> 1) ^ f;
    voffO = (4 * w + G) * 1024 + (128 * (g >> 1) + cb * 16) * 2 + (lane & 1) * 16; }
  const u32 ldw = lds0 + w * 1024;
  auto dmaX = [&](int m) { dma(ldw + (m >> 1) * 16384 + (m & 1) * 8192, voffX, rsX, (m & 1) * 65536 + (((m >> 1) + rot) & 7) * 128); };
  auto dmaW = [&](int n) { dma(ldw + WR + (n & 3) * 8192, voffW, rsW, ((((n >> 1) + rot) & 7) * 64 + (n & 1) * 32) * 3072); };
  auto dmaQ = [&](int m) { dma(ldw + WR + (m & 3) * 8192, voffQ, rsW, ((m + rot) & 7) * 64 * 3072); };
  auto dmaO = [&](int i, int u, u32 vo, u32 extra) { dma(ldw + i * 16384 + u * 8192 + extra, vo, rsO, (64 * i + 32 * u) * 1024); };

  dmaX(0);
  sfor<4>([&](auto n) { dmaW(n); dmaX(n + 1); });
  const u64 E = *(const AS4 u64*)(ws + WS_EPOCH + br * 256);
  const u64 tag = E + 1;
  asm volatile("" ::"s"(tag));

  const u32 rh = w & 1, hh = w >> 1;
  const u32 xa0 = lds0 + (64 * rh + l15) * 128 + ((G ^ (l15 >> 1)) * 16);
  const u32 xa1 = xa0 ^ 64;
  const u32 wk = lds0 + WR + (8 * G + q4) * 256 + ((hh ^ (q4 | ((G & 1) << 2))) * 32) + p4 * 8;
  const u32 wv = wk ^ 128;
  f32x4 ak[4], av[4];
  sfor<4>([&](auto b) { ak[b] = (f32x4){0, 0, 0, 0}; av[b] = (f32x4){0, 0, 0, 0}; });
  u32x4 xf[2][4];
  u32x2 kf[2][2], vf[2][2];
  auto rdK = [&](auto i) { constexpr int I = i, B = I & 1; kf[B][0] = ldsr_tr(wk + (I & 3) * 8192); kf[B][1] = ldsr_tr(wk + (I & 3) * 8192 + 1024); };
  auto rdV = [&](auto i) { constexpr int I = i, B = I & 1; vf[B][0] = ldsr_tr(wv + (I & 3) * 8192); vf[B][1] = ldsr_tr(wv + (I & 3) * 8192 + 1024); };
  auto rdX = [&](auto i, auto b) { constexpr int I = i, B = I & 1; xf[B][b] = ldsr128((I & 1 ? xa1 : xa0) + (I >> 1) * 16384 + b * 2048); };
  using std::integral_constant;
#define IC(n) integral_constant<int, (n)>{}
  wait_vm_bar<6>();
  STAMP(1); CSTAMP(1);
  rdK(IC(0)); rdX(IC(0), IC(0)); rdX(IC(0), IC(1)); rdX(IC(0), IC(2)); rdX(IC(0), IC(3)); rdV(IC(0));
  sfor<16>([&](auto i) {
    constexpr int I = i, B = I & 1, n = I + 4;
    constexpr bool RD = I < 15;
    if constexpr (I <= 11) wait_vm_bar<4>();
    else if constexpr (I == 12) wait_vm_bar<3>();
    else if constexpr (I <= 14) wait_vm_bar<2>();
    else lgk_bar();
    SB();
    const bf16x8 kb = mk8(kf[B][0], kf[B][1]), vb = mk8(vf[B][0], vf[B][1]);
    ak[0] = mfma32(asb8(xf[B][0]), kb, ak[0]); SB();
    if constexpr (RD) { rdK(IC(I + 1)); SB(); }
    ak[1] = mfma32(asb8(xf[B][1]), kb, ak[1]); SB();
    if constexpr (RD) { rdX(IC(I + 1), IC(0)); SB(); }
    ak[2] = mfma32(asb8(xf[B][2]), kb, ak[2]); SB();
    if constexpr (RD) { rdX(IC(I + 1), IC(1)); SB(); }
    ak[3] = mfma32(asb8(xf[B][3]), kb, ak[3]); SB();
    if constexpr (RD) { rdX(IC(I + 1), IC(2)); SB(); }
    av[0] = mfma32(asb8(xf[B][0]), vb, av[0]); SB();
    if constexpr (RD) { rdX(IC(I + 1), IC(3)); SB(); }
    av[1] = mfma32(asb8(xf[B][1]), vb, av[1]); SB();
    if constexpr (RD) { rdV(IC(I + 1)); SB(); }
    av[2] = mfma32(asb8(xf[B][2]), vb, av[2]); SB();
    if constexpr (n <= 15) dmaW(n); else if constexpr (n <= 18) dmaQ(n - 16);
    SB();
    av[3] = mfma32(asb8(xf[B][3]), vb, av[3]); SB();
    if constexpr (n <= 14) dmaX(n + 1);
    SB();
  });
  STAMP(2); CSTAMP(2);
  f32x4 kvp = {0, 0, 0, 0}, zp = {0, 0, 0, 0};
  {
    const u32x4 ones = {0x3f803f80u, 0x3f803f80u, 0x3f803f80u, 0x3f803f80u};
    sfor<2>([&](auto h) {
      u32x4 pk = {phi2(ak[2 * h][0], ak[2 * h][1]), phi2(ak[2 * h][2], ak[2 * h][3]), phi2(ak[2 * h + 1][0], ak[2 * h + 1][1]), phi2(ak[2 * h + 1][2], ak[2 * h + 1][3])};
      u32x4 vv = {cvtpk(av[2 * h][0], av[2 * h][1]), cvtpk(av[2 * h][2], av[2 * h][3]), cvtpk(av[2 * h + 1][0], av[2 * h + 1][1]), cvtpk(av[2 * h + 1][2], av[2 * h + 1][3])};
      kvp = mfma32(asb8(pk), asb8(vv), kvp);
      zp = mfma32(asb8(pk), asb8(ones), zp);
    });
  }
  {
    const u32 sc = lds0 + WR + 3 * 8192 + hh * 2048 + lane * 16;
    *(AS3 f32x4*)(sc + (rh ? 0 : 1024)) = rh ? kvp : zp;
    lgk_bar();
    f32x4 o = *(const AS3 f32x4*)(sc + (rh ? 1024 : 0));
    f32x4 mine = rh ? zp : kvp;
    f32x4 s = rh ? (o + mine) : (mine + o);
    const u32 pbase = WS_PART + ((br * 8 + g) * 16 + r) * PART_BYTES;
    u32 off = pbase + (rh ? (4096 + hh * 64 + G * 16) : (hh * 1024 + lane * 16));
    if (rh && l15 != 0) off = 0x7fffffffu - 64;
    __builtin_amdgcn_raw_buffer_store_b128(__builtin_bit_cast(u32x4, s), rsS, off, 0, 16);
  }
  STAMP(3);
  const u32 rq = w & 3, hp = w >> 2;
  const u32 xb0 = lds0 + (32 * rq + l15) * 128 + ((G ^ (l15 >> 1)) * 16), xb1 = xb0 ^ 64;
  const u32 wq0 = lds0 + WR + (8 * G + q4) * 128 + (((2 * hp) ^ (((q4 >> 1) & 1) | ((G & 1) << 1))) * 32) + p4 * 8, wq1 = wq0 ^ 32;
  f32x4 aq[2][2];
  sfor<2>([&](auto b) { aq[b][0] = (f32x4){0, 0, 0, 0}; aq[b][1] = (f32x4){0, 0, 0, 0}; });
  u32x4 xq[2][2][2];
  u32x2 qf[2][2][2][2];
  auto rdQ = [&](auto i, auto kh, auto h) { constexpr int I = i, B = I & 1; const u32 a = (h ? wq1 : wq0) + (I & 3) * 8192 + kh * 4096;
    qf[B][kh][h][0] = ldsr_tr(a); qf[B][kh][h][1] = ldsr_tr(a + 512); };
  auto rdY = [&](auto i, auto kh, auto b) { constexpr int I = i, B = I & 1; xq[B][kh][b] = ldsr128((kh ? xb1 : xb0) + I * 16384 + b * 2048); };
  wait_vm_bar<3>();
  dmaQ(3);
  sfor<2>([&](auto kh) { rdQ(IC(0), kh, IC(0)); rdY(IC(0), kh, IC(0)); rdY(IC(0), kh, IC(1)); rdQ(IC(0), kh, IC(1)); });
  sfor<8>([&](auto i) {
    constexpr int I = i, B = I & 1;
    constexpr bool RD = I < 7;
    if constexpr (I == 0) wait_vm_bar<3>();
    else if constexpr (I == 1) wait_vm_bar<5>();
    else if constexpr (I == 2) wait_vm_bar<6>();
    else if constexpr (I == 3 || I == 4) wait_vm_bar<8>();
    else if constexpr (I == 5) wait_vm_bar<5>();
    else if constexpr (I == 6) wait_vm_bar<2>();
    SB();
    if constexpr (I == 2) { if (w == 0 && lane == 0) __hip_atomic_store((AS1 u64*)(ws + WS_F1) + (br * 8 + g) * 16 + r, tag, RLX_AGENT); STAMP(4); SB(); }
    auto mm = [&](auto kh, auto h, auto b) { aq[b][h] = mfma32(mk8(qf[B][kh][h][0], qf[B][kh][h][1]), asb8(xq[B][kh][b]), aq[b][h]); SB(); };
    mm(IC(0), IC(0), IC(0));
    if constexpr (RD) { rdQ(IC(I + 1), IC(0), IC(0)); SB(); }
    mm(IC(0), IC(0), IC(1));
    if constexpr (RD) { rdY(IC(I + 1), IC(0), IC(0)); rdY(IC(I + 1), IC(0), IC(1)); SB(); }
    mm(IC(0), IC(1), IC(0));
    if constexpr (RD) { rdQ(IC(I + 1), IC(0), IC(1)); SB(); }
    mm(IC(0), IC(1), IC(1));
    if constexpr (RD) { rdQ(IC(I + 1), IC(1), IC(0)); SB(); }
    mm(IC(1), IC(0), IC(0));
    if constexpr (RD) { rdY(IC(I + 1), IC(1), IC(0)); rdY(IC(I + 1), IC(1), IC(1)); SB(); }
    mm(IC(1), IC(0), IC(1));
    if constexpr (RD) { rdQ(IC(I + 1), IC(1), IC(1)); SB(); }
    if constexpr (I <= 3) { dmaQ(I + 4); SB(); }
    mm(IC(1), IC(1), IC(0));
    if constexpr (I <= 3) { dmaO(I, 0, voffO, 0); SB(); }
    mm(IC(1), IC(1), IC(1));
    if constexpr (I <= 3) { dmaO(I, 1, voffO, 0); SB(); }
  });
  STAMP(5);
  u32x2 pq[2][2];
  sfor<2>([&](auto b) { sfor<2>([&](auto h) { pq[b][h] = (u32x2){phi2(aq[b][h][0], aq[b][h][1]), phi2(aq[b][h][2], aq[b][h][3])}; }); });
  if (w == 0) poll_flags((const u64*)(ws + WS_F1) + (br * 8 + g) * 16, 16, tag, lane);
  lgk_bar();
  STAMP(6);
  {
    const u32 gh = w >> 1, half = w & 1;
    const u32 pb = WS_PART + ((br * 8 + g) * 16 + half * 8) * PART_BYTES;
    u32x4 pv[8];
    sfor<8>([&](auto p) { pv[p] = __builtin_amdgcn_raw_buffer_load_b128(rsS, gh * 1024 + lane * 16, pb + p * PART_BYTES, 16); });
    u32x2 zv = __builtin_amdgcn_raw_buffer_load_b64(rsS, 4096 + gh * 64 + (lane & 7) * 8 + (lane >> 3) * PART_BYTES, pb, 16);
    f32x4 s = __builtin_bit_cast(f32x4, pv[0]);
    sfor<7>([&](auto p) { s += __builtin_bit_cast(f32x4, pv[p + 1]); });
    float z0 = __uint_as_float(zv[0]), z1 = __uint_as_float(zv[1]);
    auto red = [&](float x) {
      x += __uint_as_float(__builtin_amdgcn_update_dpp(0u, __float_as_uint(x), 0x128, 0xf, 0xf, false));
      auto a = __builtin_amdgcn_permlane16_swap(__float_as_uint(x), __float_as_uint(x), false, false);
      x = __uint_as_float(a[0]) + __uint_as_float(a[1]);
      auto c = __builtin_amdgcn_permlane32_swap(__float_as_uint(x), __float_as_uint(x), false, false);
      return __uint_as_float(c[0]) + __uint_as_float(c[1]);
    };
    z0 = red(z0); z1 = red(z1);
    const u32 sc = lds0 + WR + w * 2048;
    *(AS3 f32x4*)(sc + lane * 16) = s;
    if (lane < 8) *(AS3 f32x2*)(sc + 1024 + lane * 8) = (f32x2){z0, z1};
  }
  lgk_bar();
  STAMP(7);
  {
    sfor<2>([&](auto h) {
      const u32 sc = lds0 + WR + (2 * (2 * hp + h)) * 2048;
      f32x4 k0 = *(const AS3 f32x4*)(sc + lane * 16), k1 = *(const AS3 f32x4*)(sc + 2048 + lane * 16);
      f32x4 z0 = *(const AS3 f32x4*)(sc + 1024 + G * 16), z1 = *(const AS3 f32x4*)(sc + 2048 + 1024 + G * 16);
      k0 += k1; z0 += z1;
      u32x2 kvA = {cvtpk(k0[0], k0[1]), cvtpk(k0[2], k0[3])}, zA = {cvtpk(z0[0], z0[1]), cvtpk(z0[2], z0[3])};
      sfor<2>([&](auto b) {
        f32x4 num = mfma16(kvA, pq[b][h], (f32x4){0, 0, 0, 0});
        f32x4 den = mfma16(zA, pq[b][h], (f32x4){0, 0, 0, 0});
        u32 d1 = cvtpk(den[0], den[0]);
        u32 d2 = cvtpk(lo16(d1) + 1e-6f, 0.f);
        float rd = __builtin_amdgcn_rcpf(lo16(d2));
        u32 n0 = cvtpk(num[0], num[1]), n1 = cvtpk(num[2], num[3]);
        pq[b][h] = (u32x2){cvtpk(lo16(n0) * rd, hi16(n0) * rd), cvtpk(lo16(n1) * rd, hi16(n1) * rd)};
      });
    });
    sfor<2>([&](auto b) {
      u32x4 v = {pq[b][0][0], pq[b][0][1], pq[b][1][0], pq[b][1][1]};
      __builtin_amdgcn_raw_buffer_store_b128(v, rsS, lane * 16 + WS_O + ((br * 128 + r * 8 + 2 * rq + b) * 16 + g * 2 + hp) * 1024, 0, 16);
    });
  }
  wait_vm_bar<0>();
  STAMP(8);
  if (w == 0 && lane == 0) __hip_atomic_store((AS1 u64*)(ws + WS_F2) + (br * 16 + r) * 16 + g, tag, RLX_AGENT);
  if (w >= 4) {
    const u32 vo = voffO - 16 * 1024;
    sfor<4>([&](auto i) {
      sfor<2>([&](auto u) {
        dmaO(4 + i, u, vo, (u32)(-4096));
        dmaO(4 + i, u, voffO, 0);
      });
    });
    wait_vm<0>();
    asm volatile("s_barrier" ::: "memory");
    return;
  }
  poll_flags((const u64*)(ws + WS_F2) + (br * 16 + r) * 16, 8, tag, lane);
  STAMP(9); CSTAMP(9);
  u32x4 of[16];
  {
    const u32 ob = WS_O + ((br * 128 + r * 8 + 4 * (g & 1) + w) * 16) * 1024;
    sfor<16>([&](auto t) { of[t] = __builtin_amdgcn_raw_buffer_load_b128(rsS, lane * 16, ob + t * 1024, 16); });
  }
  if (r == 0 && g == 0 && w == 0 && lane == 0) __hip_atomic_store((AS1 u64*)(ws + WS_EPOCH + br * 256), tag, RLX_AGENT);
  asm volatile("s_barrier" ::: "memory");
  f32x4 ao[8];
  sfor<8>([&](auto c) { ao[c] = (f32x4){0, 0, 0, 0}; });
  {
    const u32 rr = 4 * G + q4;
    const u32 wb = lds0 + rr * 256 + (rr & 7) * 32 + p4 * 8;
    constexpr int D = 6;
    u32x2 af[8][2];
    auto rdO = [&](auto s_) { constexpr int S = s_, t = S >> 3, c = S & 7; const u32 a = (wb ^ (c * 32)) + t * 8192;
      af[S & 7][0] = ldsr_tr(a); af[S & 7][1] = ldsr_tr(a + 4096); };
    sfor<D>([&](auto s_) { rdO(s_); });
    SB();
    sfor<128>([&](auto s_) {
      constexpr int S = s_, t = S >> 3, c = S & 7;
      if constexpr (S + D < 128) { rdO(IC(S + D)); SB(); }
      ao[c] = mfma32(mk8(af[S & 7][0], af[S & 7][1]), asb8(of[t]), ao[c]); SB();
    });
  }
  STAMP(10); CSTAMP(10);
  {
    const __amdgpu_buffer_rsrc_t rsU = __builtin_amdgcn_make_buffer_rsrc(outp, 0, 2048 * 1024 * 2, SRD3);
    const u32 op = (r * 128 + 64 * (g & 1) + 16 * w + l15) * 2048 + br * 1024 + (g >> 1) * 256 + (G & 1) * 32 + (G >> 1) * 16;
    sfor<4>([&](auto m) {
      u32 a0 = cvtpk(ao[2 * m][0], ao[2 * m][1]), a1 = cvtpk(ao[2 * m][2], ao[2 * m][3]);
      u32 b0 = cvtpk(ao[2 * m + 1][0], ao[2 * m + 1][1]), b1 = cvtpk(ao[2 * m + 1][2], ao[2 * m + 1][3]);
      auto s0 = __builtin_amdgcn_permlane16_swap(a0, b0, false, false);
      auto s1 = __builtin_amdgcn_permlane16_swap(a1, b1, false, false);
      u32x4 v = {s0[0], s1[0], s0[1], s1[1]};
      __builtin_amdgcn_raw_buffer_store_b128(v, rsU, op + m * 64, 0, 16);
    });
  }
  STAMP(11);
}

extern "C" void kernel_launch(void* const* d_in, const int* in_sizes, int n_in, void* d_out, int out_size, void* d_ws, size_t ws_size, hipStream_t stream) {
  (void)in_sizes; (void)n_in; (void)out_size; (void)ws_size;
  hipFuncSetAttribute((const void*)fused_linattn_kernel, hipFuncAttributeMaxDynamicSharedMemorySize, LDS_BYTES);
  fused_linattn_kernel<<<256, 512, LDS_BYTES, stream>>>(d_in[0], d_in[1], d_in[2], d_in[3], d_in[4], d_in[5], d_out, (char*)d_ws
  );
}
```

```cpp
#include <hip/hip_runtime.h>
#include <stdint.h>
#include <utility>

typedef unsigned u32;
typedef unsigned long long u64;
typedef u32 u32x4 __attribute__((ext_vector_type(4)));
typedef u32 u32x2 __attribute__((ext_vector_type(2)));
typedef float f32x4 __attribute__((ext_vector_type(4)));
typedef float f32x2 __attribute__((ext_vector_type(2)));
typedef short s16x4 __attribute__((ext_vector_type(4)));
typedef __bf16 bf16x8 __attribute__((ext_vector_type(8)));
typedef __bf16 bf16x2 __attribute__((ext_vector_type(2)));
#define AS3 __attribute__((address_space(3)))
#define AS1 __attribute__((address_space(1)))
#define AS4 __attribute__((address_space(4)))
#define DI __device__ __forceinline__
#define SB() __builtin_amdgcn_sched_barrier(0)
#define RLX_AGENT __ATOMIC_RELAXED, __HIP_MEMORY_SCOPE_AGENT

constexpr int SRD3 = 0x00020000;
constexpr u32 PG = 16384;
constexpr u32 LDS_BYTES = 163840;
constexpr u32 PX[8] = {1, 3, 5, 7, 9, 2, 6, 0};
constexpr u32 PW[8] = {0, 2, 4, 6, 8, 0, 4, 8};
constexpr u32 QS[8] = {4 * PG, 4 * PG + 8192, 8 * PG, 8 * PG + 8192, 1 * PG, 1 * PG + 8192, 3 * PG, 3 * PG + 8192};
constexpr u32 PO[8] = {5, 7, 8, 9, 2, 1, 6, 0};
constexpr u32 XK = 4 * PG;
constexpr u32 WS_EPOCH = 0;
constexpr u32 WS_F1 = 4096;
constexpr u32 WS_F2 = 4096 + 8192;
constexpr u32 WS_PART = 65536;
constexpr u32 PART_BYTES = 2304;
constexpr u32 WS_O = 2u << 20;
constexpr u32 WS_USED = 6u << 20;
constexpr u32 SPIN_CAP = 1u << 16;
#define CLR_AT 6
#define OCLR 2
#define PREF 1
#define FLAG0_AT 14
constexpr u32 SENT = 0xffffffffu;

template <int... Is, class F> DI void sfor_impl(std::integer_sequence<int, Is...>, F&& f) { (f(std::integral_constant<int, Is>{}), ...); }
template <int N, class F> DI void sfor(F&& f) { sfor_impl(std::make_integer_sequence<int, N>{}, f); }

DI u32 rfl(u32 x) { return __builtin_amdgcn_readfirstlane(x); }
DI u32 cvtpk(float lo, float hi) { f32x2 v = {lo, hi}; bf16x2 b = __builtin_convertvector(v, bf16x2); return __builtin_bit_cast(u32, b); }
DI float lo16(u32 p) { return __uint_as_float(p << 16); }
DI float hi16(u32 p) { return __uint_as_float(p & 0xffff0000u); }
DI float phi1(float t) { return __builtin_fmaxf(t + 1.0f, __builtin_amdgcn_fmed3f(__builtin_amdgcn_exp2f(t * 1.44269504088896340736f), 0.f, 1.f)) + 1e-6f; }
DI u32 phi2(float a, float b) { return cvtpk(phi1(a), phi1(b)); }
DI void dma(u32 ldsdst, u32 voff, __amdgpu_buffer_rsrc_t srd, u32 soff) {
  asm volatile("s_mov_b32 m0, %0\n\ts_nop 0\n\tbuffer_load_dwordx4 %1, %2, %3 offen lds" ::"s"(ldsdst), "v"(voff), "s"(srd), "s"(soff) : "m0", "memory");
}
DI void dma_sc1(u32 ldsdst, u32 voff, __amdgpu_buffer_rsrc_t srd, u32 soff) {
  asm volatile("s_mov_b32 m0, %0\n\ts_nop 0\n\tbuffer_load_dwordx4 %1, %2, %3 offen sc1 lds" ::"s"(ldsdst), "v"(voff), "s"(srd), "s"(soff) : "m0", "memory");
}
DI u32x4 ld_sc1(u32 voff, __amdgpu_buffer_rsrc_t srd, u32 soff) {
  u32x4 v; asm volatile("buffer_load_dwordx4 %0, %1, %2, %3 offen sc1" : "=v"(v) : "v"(voff), "s"(srd), "s"(soff) : "memory"); return v;
}
DI u32 umax(u32 a, u32 b) { return a > b ? a : b; }
DI u32 umax4(u32x4 v) { return umax(umax(v[0], v[1]), umax(v[2], v[3])); }
DI u32x4 clean(u32x4 v) { return __builtin_elementwise_min(v, (u32x4){SENT - 1, SENT - 1, SENT - 1, SENT - 1}); }
template <int N> DI void wait_vm() { asm volatile("s_waitcnt vmcnt(%0)" ::"n"(N) : "memory"); }
template <int N> DI void wait_vm_bar() { asm volatile("s_waitcnt vmcnt(%0)\n\ts_barrier" ::"n"(N) : "memory"); }
template <int N> DI void wait_lgkm() { __builtin_amdgcn_s_waitcnt(0xC07F | (N << 8)); }
DI u32 opaque(u32 x) { asm volatile("" : "+v"(x)); return x; }
DI void pin(u32x2& a, u32x2& b) { asm volatile("" : "+v"(a), "+v"(b)); }
DI void lgk_bar() { asm volatile("s_waitcnt lgkmcnt(0)\n\ts_barrier" ::: "memory"); }
DI void bar() { asm volatile("s_barrier" ::: "memory"); }
DI u32x4 ldsr128(u32 a) { return *(const AS3 u32x4*)(a); }
DI u32x2 ldsr64(u32 a) { return *(const AS3 u32x2*)(a); }
DI u32x2 ldsr_tr(u32 a) { return __builtin_bit_cast(u32x2, __builtin_amdgcn_ds_read_tr16_b64_v4i16((AS3 s16x4*)(a))); }
DI bf16x8 mk8(u32x2 a, u32x2 b) { u32x4 v = {a[0], a[1], b[0], b[1]}; return __builtin_bit_cast(bf16x8, v); }
DI bf16x8 asb8(u32x4 v) { return __builtin_bit_cast(bf16x8, v); }
DI f32x4 mfma32(bf16x8 a, bf16x8 b, f32x4 c) { return __builtin_amdgcn_mfma_f32_16x16x32_bf16(a, b, c, 0, 0, 0); }
DI f32x4 mfma16(u32x2 a, u32x2 b, f32x4 c) { return __builtin_amdgcn_mfma_f32_16x16x16bf16_1k(__builtin_bit_cast(s16x4, a), __builtin_bit_cast(s16x4, b), c, 0, 0, 0); }

#define STAMP(i) do {} while (0)
#define CSTAMP(i) do {} while (0)

DI void poll_flags(const u64* flag, u64 want) {
  const AS1 u64* p = (const AS1 u64*)flag;
#pragma unroll 1
  for (u32 spins = 0; spins < SPIN_CAP; ++spins) {
    u64 v = __hip_atomic_load(p, RLX_AGENT);
    if (__all(v == want)) break;
    __builtin_amdgcn_s_sleep(1);
  }
  asm volatile("" ::: "memory");
}

using std::integral_constant;
#define IC(n) integral_constant<int, (n)>{}

__global__ __launch_bounds__(512, 2) void fused_linattn_kernel(const void* __restrict__ xA, const void* __restrict__ wA, const void* __restrict__ oA,
                                                               const void* __restrict__ xB, const void* __restrict__ wB, const void* __restrict__ oB,
                                                               void* __restrict__ outp, char* __restrict__ ws
) {
  extern __shared__ __attribute__((aligned(16))) char sm[];
  const u32 lds0 = (u32)(uintptr_t)(AS3 char*)sm;
  const int lane = threadIdx.x & 63;
  const u32 w = rfl(threadIdx.x >> 6);
  const u32 cw = w & 3;
  const u32 bid = blockIdx.x, xcd = bid & 7, j = bid >> 3;
  const u32 br = xcd & 1, r = ((xcd >> 1) & 1) * 8 + (j & 7), g = (xcd >> 2) * 4 + (j >> 3);
  const u32 rot = ((j & 7) + 2 * (j >> 3)) & 7;
  const u32 G = lane >> 4, l15 = lane & 15, q4 = (lane >> 2) & 3, p4 = lane & 3;
  STAMP(0);
  const __amdgpu_buffer_rsrc_t rsS = __builtin_amdgcn_make_buffer_rsrc((void*)ws, 0, WS_USED, SRD3);
  const u64 E = *(const AS4 u64*)(ws + WS_EPOCH + br * 256);
  const __amdgpu_buffer_rsrc_t rsX = __builtin_amdgcn_make_buffer_rsrc((void*)(br ? xB : xA), 0, 2048 * 512 * 2, SRD3);
  const __amdgpu_buffer_rsrc_t rsW = __builtin_amdgcn_make_buffer_rsrc((void*)(br ? wB : wA), 0, 512 * 1536 * 2, SRD3);
  const __amdgpu_buffer_rsrc_t rsO = __builtin_amdgcn_make_buffer_rsrc((void*)(br ? oB : oA), 0, 512 * 512 * 2, SRD3);

  if (w >= 4) {
    const u32 voffX = (r * 128 + 8 * cw + (lane >> 3)) * 1024 + (((lane & 7) ^ (4 * (cw & 1) + G)) * 16);
    u32 voffW;
    { u32 f = G | (((cw >> 1) & 1) << 2), cb = (l15 >> 1) ^ f;
      voffW = (4 * cw + G) * 3072 + (512 + g * 64 + (cb & 3) * 16 + (cb >> 2) * 512) * 2 + (lane & 1) * 16; }
    const u32 ldw = lds0 + cw * 1024;
    auto dmaX = [&](int s) { const u32 so = ((s + rot) & 7) * 128;
      sfor<4>([&](auto i) { dma(ldw + PX[s] * PG + i * 4096, voffX, rsX, so + i * 32768); }); };
    auto dmaW = [&](int s) { const u32 so = ((s + rot) & 7) * 64 * 3072;
      sfor<4>([&](auto i) { dma(ldw + PW[s] * PG + i * 4096, voffW, rsW, so + i * 16 * 3072); }); };
    dmaW(0); dmaX(0); dmaW(1); dmaX(1); dmaW(2); dmaX(2);
    asm volatile("" ::"s"(E));
    u32 voffQ;
    { u32 f = (G & 1) | ((cw & 1) << 1), cb = ((lane & 7) >> 1) ^ f;
      voffQ = (8 * cw + (lane >> 3)) * 3072 + (g * 64 + cb * 16) * 2 + (lane & 1) * 16; }
    u32 voffO;
    { u32 f = 4 * (cw & 1) + G, cb = (l15 >> 1) ^ f;
      voffO = (4 * cw + G) * 1024 + (128 * (g >> 1) + cb * 16) * 2 + (lane & 1) * 16; }
    auto dmaQ = [&](int t) { const u32 so = ((t + rot) & 7) * 64 * 3072;
      sfor<2>([&](auto i) { dma(ldw + QS[t] + i * 4096, voffQ, rsW, so + i * 32 * 3072); }); };
    auto dmaO = [&](int i) { sfor<4>([&](auto u) { dma(ldw + PO[i] * PG + u * 4096, voffO, rsO, (64 * i + 16 * u) * 1024); }); };
    wait_vm_bar<16>();
    STAMP(1);
    dmaW(3); dmaX(3);
    wait_vm_bar<16>(); dmaW(4); dmaX(4); dmaW(5);
    wait_vm_bar<20>(); dmaX(5);
    wait_vm_bar<16>(); dmaW(6);
    wait_vm_bar<12>(); dmaX(6);
    wait_vm_bar<8>(); dmaW(7);
    wait_vm_bar<4>(); dmaX(7);
    wait_vm_bar<0>(); dmaQ(0); dmaQ(1);
    bar(); dmaQ(2); dmaQ(3);
    STAMP(2);
    const u64 tag = E + 1;
    const u32 fi = br * 512 + (g * 4 + cw) * 16 + l15;
    const u32 fo = br * 512 + ((2 * cw + ((lane >> 1) & 1)) * 4 + (g & 1) + 2 * (lane & 1)) * 16 + r;
    wait_vm_bar<0>();
    STAMP(3);
    bar(); dmaQ(4); dmaQ(5);
    bar();
    u64 fl;
    asm volatile("buffer_load_dwordx2 %0, %1, %2, 0 offen sc1" : "=v"(fl) : "v"(WS_F1 + fi * 8), "s"(rsS) : "memory");
    dmaQ(6); dmaQ(7);
    wait_vm_bar<5>();
    dmaO(0); dmaO(1);
    asm volatile("s_waitcnt vmcnt(8)\n\ts_barrier" : "+v"(fl)::"memory");
    if (!__all(fl == tag)) poll_flags((const u64*)(ws + WS_F1) + fi, tag);
    STAMP(4);
    {
      const u32 pb = WS_PART + (br * 8 + g) * 16 * PART_BYTES;
      const u32 vk = G * PART_BYTES + cw * 512 + l15 * 16, vz = 2048 + cw * 64 + (lane & 3) * 16 + (lane >> 2) * PART_BYTES;
      u32x4 pv[8], zv;
      sfor<8>([&](auto p) { pv[p] = ld_sc1(vk, rsS, pb + (p >> 1) * 4 * PART_BYTES + (p & 1) * 256); });
      zv = ld_sc1(vz, rsS, pb);
      dmaO(2); dmaO(3);
      const u32 one = (l15 & 1) ? 0x3f800000u : 0x3f80u, jd = (l15 >> 1) & 3;
      const u32x4 sel = {jd == 0 ? one : 0u, jd == 1 ? one : 0u, jd == 2 ? one : 0u, jd == 3 ? one : 0u};
      const u32x4 zero4 = {0, 0, 0, 0};
      const u32x4 selLo = l15 < 8 ? sel : zero4, selHi = l15 < 8 ? zero4 : sel;
      asm volatile("s_waitcnt vmcnt(8)" : "+v"(pv[0]), "+v"(pv[1]), "+v"(pv[2]), "+v"(pv[3]), "+v"(pv[4]), "+v"(pv[5]), "+v"(pv[6]), "+v"(pv[7]), "+v"(zv));
      STAMP(5);
      f32x4 s0; float z0, z1, z2, z3;
      auto red = [&](float x) {
        x += __uint_as_float(__builtin_amdgcn_update_dpp(0u, __float_as_uint(x), 0x124, 0xf, 0xf, false));
        x += __uint_as_float(__builtin_amdgcn_update_dpp(0u, __float_as_uint(x), 0x128, 0xf, 0xf, false));
        auto a = __builtin_amdgcn_permlane16_swap(__float_as_uint(x), __float_as_uint(x), false, false);
        x = __uint_as_float(a[0]) + __uint_as_float(a[1]);
        auto c = __builtin_amdgcn_permlane32_swap(__float_as_uint(x), __float_as_uint(x), false, false);
        return __uint_as_float(c[0]) + __uint_as_float(c[1]);
      };
      auto reduce = [&]() {
        s0 = (f32x4){0, 0, 0, 0};
        f32x4 s1 = {0, 0, 0, 0};
        sfor<4>([&](auto p) { s0 = mfma32(asb8(selLo), asb8(pv[2 * p]), s0); s1 = mfma32(asb8(selHi), asb8(pv[2 * p + 1]), s1); });
        s0 += s1;
        f32x4 z = __builtin_bit_cast(f32x4, zv);
        z0 = red(z[0]); z1 = red(z[1]); z2 = red(z[2]); z3 = red(z[3]);
      };
      reduce();
      if (__any(__builtin_isunordered(s0[0], s0[1]) || __builtin_isunordered(s0[2], s0[3]) || __builtin_isunordered(z0, z1) || __builtin_isunordered(z2, z3))) {
#pragma unroll 1
        for (u32 spins = 0; spins < SPIN_CAP; ++spins) {
          u32 pend = 0;
          sfor<8>([&](auto p) { if (__any(umax4(pv[p]) == SENT)) { pend = 1; pv[p] = __builtin_amdgcn_raw_buffer_load_b128(rsS, vk, pb + (p >> 1) * 4 * PART_BYTES + (p & 1) * 256, 16); } });
          if (__any(umax4(zv) == SENT)) { pend = 1; zv = __builtin_amdgcn_raw_buffer_load_b128(rsS, vz, pb, 16); }
          asm volatile("" ::: "memory");
          if (!pend) break;
        }
        reduce();
      }
      const u32 sc = lds0 + XK + cw * 1024;
      *(AS3 u32x2*)(sc + lane * 8) = (u32x2){cvtpk(s0[0], s0[1]), cvtpk(s0[2], s0[3])};
      if (lane < 4) *(AS3 u32x2*)(sc + 512 + lane * 8) = (u32x2){cvtpk(z0, z1), cvtpk(z2, z3)};
    }
    STAMP(6);
    dmaO(4); dmaO(5);
    lgk_bar();
    asm volatile("buffer_load_dwordx2 %0, %1, %2, 0 offen sc1" : "=v"(fl) : "v"(WS_F2 + fo * 8), "s"(rsS) : "memory");
    dmaO(6); dmaO(7);
    STAMP(7);
    asm volatile("s_waitcnt vmcnt(0)" : "+v"(fl)::"memory");
    if (!__all(fl == tag)) poll_flags((const u64*)(ws + WS_F2) + fo, tag);
    bar();
  } else {
    __builtin_amdgcn_s_setprio(1);
    const u32 xa0 = lds0 + l15 * 128 + ((G ^ (l15 >> 1)) * 16), xa1 = xa0 ^ 64;
    const u32 wk = lds0 + (8 * G + q4) * 256 + ((cw ^ (q4 | ((G & 1) << 2))) * 32) + p4 * 8;
    const u32 wv = wk ^ 128;
    const u32 xa0w[3] = {xa0, opaque(xa0 + 65536), opaque(xa0 + 131072)}, xa1w[3] = {xa1, opaque(xa1 + 65536), opaque(xa1 + 131072)};
    const u32 wkw[3] = {wk, opaque(wk + 65536), opaque(wk + 131072)}, wvw[3] = {wv, opaque(wv + 65536), opaque(wv + 131072)};
    f32x4 ak[8], av[8];
    sfor<8>([&](auto b) { ak[b] = (f32x4){0, 0, 0, 0}; av[b] = (f32x4){0, 0, 0, 0}; });
    u32x4 xf[2][8];
    u32x2 kf[2][2], vf[2][2];
    auto rdW = [&](auto i, auto c) { constexpr int I = i, C = c, Bf = I & 1; constexpr u32 o = PW[(I >> 1) & 7] * PG + (I & 1) * 8192 + (C & 1) * 1024;
      if constexpr (C < 2) kf[Bf][C] = ldsr_tr(wkw[o >> 16] + (o & 0xffff)); else vf[Bf][C & 1] = ldsr_tr(wvw[o >> 16] + (o & 0xffff)); };
    auto rdX = [&](auto i, auto b) { constexpr int I = i, Bf = I & 1; constexpr u32 o = PX[(I >> 1) & 7] * PG + b * 2048;
      xf[Bf][b] = ldsr128((I & 1 ? xa1w : xa0w)[o >> 16] + (o & 0xffff)); };
    const u32 rh = cw & 1, hp = cw >> 1;
    const u32 pbase = WS_PART + ((br * 8 + g) * 16 + r) * PART_BYTES;
    const u32 vpk = (G & 1) ? 0x7fffffc0u : pbase + cw * 512 + (G >> 1) * 256 + l15 * 16, vpz = l15 ? 0x7fffffc0u : pbase + 2048 + cw * 64 + G * 16;
    const u32 vo = lane * 16 + WS_O + ((br * 128 + r * 8 + 4 * rh) * 16 + g * 2 + hp) * 1024;
    const u32x4 sent = {SENT, SENT, SENT, SENT};
    const u64 tag = E + 1;
    u32 pf = 0;
    asm volatile("" ::"s"(E));
    bar();
    STAMP(1); CSTAMP(1);
    sfor<4>([&](auto c) { rdW(IC(0), c); });
    sfor<8>([&](auto b) { rdX(IC(0), b); });
    sfor<16>([&](auto i) {
      constexpr int I = i, Bf = I & 1;
      constexpr bool RD = I < 15;
      if constexpr (I & 1) { wait_lgkm<0>(); bar(); }
      else wait_lgkm<4>();
      if constexpr (I == 4 && PREF) {
        if (cw == 0) pf = __builtin_amdgcn_raw_buffer_load_b32(rsW, ((j & 7) * 64 + lane) * 3072 + g * 128, 0, 0);
        if (cw == 1) pf = __builtin_amdgcn_raw_buffer_load_b32(rsO, (((g & 1) * 8 + (j & 7)) * 32 + (lane >> 1)) * 1024 + (g >> 1) * 256 + (lane & 1) * 128, 0, 0);
      }
      if constexpr (I == FLAG0_AT) {
        wait_vm<0>();
        asm volatile("" ::"v"(pf));
        if (lane == 0) __hip_atomic_store((AS1 u64*)(ws + WS_F1) + ((br * 8 + g) * 4 + cw) * 16 + r, tag, RLX_AGENT);
      }
      SB();
      const bf16x8 kb = mk8(kf[Bf][0], kf[Bf][1]), vb = mk8(vf[Bf][0], vf[Bf][1]);
      sfor<16>([&](auto m) {
        constexpr int M = m, Bk = M >> 1;
        if constexpr (M == 8 && !(I & 1)) { wait_lgkm<(RD ? 8 : 0)>(); SB(); }
        if constexpr (M & 1) av[Bk] = mfma32(asb8(xf[Bf][Bk]), vb, av[Bk]); else ak[Bk] = mfma32(asb8(xf[Bf][Bk]), kb, ak[Bk]);
        SB();
        if constexpr (RD && M < 4) { rdW(IC(I + 1), m); SB(); }
        if constexpr (RD && M >= 4 && M < 12) { rdX(IC(I + 1), IC(M - 4)); SB(); }
        if constexpr (M == 13 && I >= CLR_AT && I < CLR_AT + 2) {
          __builtin_amdgcn_raw_buffer_store_b128(sent, rsS, I == CLR_AT ? vpk : vpz, 0, 16);
          SB();
        }
      });
    });
    STAMP(2); CSTAMP(2);
    {
      const u32x4 ones = {0x3f803f80u, 0x3f803f80u, 0x3f803f80u, 0x3f803f80u};
      f32x4 kvp = {0, 0, 0, 0}, zp = {0, 0, 0, 0};
      sfor<4>([&](auto h) {
        u32x4 pk = {phi2(ak[2 * h][0], ak[2 * h][1]), phi2(ak[2 * h][2], ak[2 * h][3]), phi2(ak[2 * h + 1][0], ak[2 * h + 1][1]), phi2(ak[2 * h + 1][2], ak[2 * h + 1][3])};
        u32x4 vv = {cvtpk(av[2 * h][0], av[2 * h][1]), cvtpk(av[2 * h][2], av[2 * h][3]), cvtpk(av[2 * h + 1][0], av[2 * h + 1][1]), cvtpk(av[2 * h + 1][2], av[2 * h + 1][3])};
        kvp = mfma32(asb8(pk), asb8(vv), kvp);
        zp = mfma32(asb8(pk), asb8(ones), zp);
        if constexpr (OCLR == 1) { __builtin_amdgcn_raw_buffer_store_b128(sent, rsS, vo + h * 16384, 0, 16); SB(); }
      });
      const u32 w0 = cvtpk(kvp[0], kvp[1]), w1 = cvtpk(kvp[2], kvp[3]);
      const auto s0 = __builtin_amdgcn_permlane16_swap(w0, w0, false, false), s1 = __builtin_amdgcn_permlane16_swap(w1, w1, false, false);
      __builtin_amdgcn_raw_buffer_store_b128(clean((u32x4){s0[0], s1[0], s0[1], s1[1]}), rsS, vpk, 0, 16);
      __builtin_amdgcn_raw_buffer_store_b128(clean(__builtin_bit_cast(u32x4, zp)), rsS, vpz, 0, 16);
      if constexpr (OCLR == 3) sfor<4>([&](auto b) { __builtin_amdgcn_raw_buffer_store_b128(sent, rsS, vo + b * 16384, 0, 16); });
    }
    STAMP(3);
    const u32 xb0 = xa0 + rh * 8192, xb1 = xb0 ^ 64;
    const u32 wq0 = lds0 + (8 * G + q4) * 128 + (((2 * hp) ^ (((q4 >> 1) & 1) | ((G & 1) << 1))) * 32) + p4 * 8, wq1 = wq0 ^ 32;
    const u32 xb0w[3] = {opaque(xb0), opaque(xb0 + 65536), opaque(xb0 + 131072)}, xb1w[3] = {opaque(xb1), opaque(xb1 + 65536), opaque(xb1 + 131072)};
    const u32 wqw[3][2] = {{opaque(wq0), opaque(wq1)}, {opaque(wq0 + 65536), opaque(wq1 + 65536)}, {opaque(wq0 + 131072), opaque(wq1 + 131072)}};
    f32x4 aq[4][2];
    sfor<4>([&](auto b) { aq[b][0] = (f32x4){0, 0, 0, 0}; aq[b][1] = (f32x4){0, 0, 0, 0}; });
    u32x4 xq[2][4];
    u32x2 qf[2][2][2];
    auto rdQ = [&](auto i, auto h, auto c) { constexpr int I = i, Bf = I & 1; constexpr u32 o = QS[(I >> 1) & 7] + (I & 1) * 4096 + c * 512;
      qf[Bf][h][c] = ldsr_tr(wqw[o >> 16][h] + (o & 0xffff)); };
    auto rdY = [&](auto i, auto b) { constexpr int I = i, Bf = I & 1; constexpr u32 o = PX[(I >> 1) & 7] * PG + b * 2048;
      xq[Bf][b] = ldsr128((I & 1 ? xb1w : xb0w)[o >> 16] + (o & 0xffff)); };
    auto rdB = [&](auto i, auto n) { constexpr int N = n; if constexpr (N < 4) rdQ(i, IC(N >> 1), IC(N & 1)); else rdY(i, IC(N - 4)); };
    bar();
    sfor<8>([&](auto n) { rdB(IC(0), n); });
    sfor<16>([&](auto i) {
      constexpr int I = i, Bf = I & 1;
      constexpr bool RD = I < 15;
      if constexpr (I == 1 || I == 3 || I == 7 || I == 11) { wait_lgkm<0>(); bar(); }
      else wait_lgkm<2>();
      SB();
      const bf16x8 q0 = mk8(qf[Bf][0][0], qf[Bf][0][1]), q1 = mk8(qf[Bf][1][0], qf[Bf][1][1]);
      sfor<8>([&](auto m) {
        constexpr int M = m, Bk = M >> 1;
        if constexpr (M == 4 && !(I == 1 || I == 3 || I == 7 || I == 11)) { wait_lgkm<(RD ? 4 : 0)>(); SB(); }
        aq[Bk][M & 1] = mfma32((M & 1) ? q1 : q0, asb8(xq[Bf][Bk]), aq[Bk][M & 1]); SB();
        if constexpr (RD) { rdB(IC(I + 1), m); SB(); }
        if constexpr (OCLR == 2 && M == 7 && (I & 3) == 1) { __builtin_amdgcn_raw_buffer_store_b128(sent, rsS, vo + (I >> 2) * 16384, 0, 16); SB(); }
        if constexpr (OCLR == 4 && M == 7 && !(I & 1) && I < 8) { __builtin_amdgcn_raw_buffer_store_b128(sent, rsS, vo + (I >> 1) * 16384, 0, 16); SB(); }
      });
    });
    STAMP(5);
    if constexpr (OCLR != 2 && OCLR != 4) {
      wait_vm<0>();
      if (lane == 0) __hip_atomic_store((AS1 u64*)(ws + WS_F2) + ((br * 8 + g) * 4 + cw) * 16 + r, tag, RLX_AGENT);
    }
    u32x2 pq[4][2];
    sfor<4>([&](auto b) { sfor<2>([&](auto h) { pq[b][h] = (u32x2){phi2(aq[b][h][0], aq[b][h][1]), phi2(aq[b][h][2], aq[b][h][3])}; }); });
    sfor<4>([&](auto b) { pin(pq[b][0], pq[b][1]); });
    if constexpr (OCLR == 2 || OCLR == 4) {
      wait_vm<0>();
      if (lane == 0) __hip_atomic_store((AS1 u64*)(ws + WS_F2) + ((br * 8 + g) * 4 + cw) * 16 + r, tag, RLX_AGENT);
    }
    STAMP(6);
    bar();
    STAMP(7);
    sfor<2>([&](auto h) {
      const u32 sc = lds0 + XK + (2 * hp + h) * 1024;
      const u32x2 kvA = ldsr64(sc + lane * 8), zA = ldsr64(sc + 512 + G * 8);
      sfor<4>([&](auto b) {
        f32x4 num = mfma16(kvA, pq[b][h], (f32x4){0, 0, 0, 0});
        f32x4 den = mfma16(zA, pq[b][h], (f32x4){0, 0, 0, 0});
        float rd = __builtin_amdgcn_rcpf(den[0] + 1e-6f);
        pq[b][h] = (u32x2){cvtpk(num[0] * rd, num[1] * rd), cvtpk(num[2] * rd, num[3] * rd)};
      });
    });
    sfor<4>([&](auto b) {
      u32x4 v = {pq[b][0][0], pq[b][0][1], pq[b][1][0], pq[b][1][1]};
      __builtin_amdgcn_raw_buffer_store_b128(clean(v), rsS, vo + b * 16384, 0, 16);
    });
    STAMP(8);
    lgk_bar();
  }
  const u64 tag = E + 1;
  const u32 wr = w >> 2, wc = w & 3;
  u32x2 af[16][2][2];
  {
    const u32 rr = 4 * G + q4;
    const u32 wb = lds0 + rr * 256 + (rr & 7) * 32 + p4 * 8;
    sfor<16>([&](auto t) { sfor<2>([&](auto c) {
      const u32 a = (wb ^ ((2 * wc + c) * 32)) + PO[t >> 1] * PG + (t & 1) * 8192;
      af[t][c][0] = ldsr_tr(a); af[t][c][1] = ldsr_tr(a + 4096); }); });
  }
  STAMP(9);
  __builtin_amdgcn_s_setprio(0);
  lgk_bar();
  {
    const u32 ob = WS_O + ((br * 128 + r * 8 + 4 * (g & 1)) * 16 + 2 * w) * 1024;
    const u32 od = lds0 + 2 * w * 4096;
    u32 pend = 0xff;
#pragma unroll 1
    for (u32 spins = 0; spins < SPIN_CAP; ++spins) {
      sfor<8>([&](auto n) { if ((pend >> n) & 1) dma_sc1(od + (n & 1) * 4096 + (n >> 1) * 1024, lane * 16, rsS, ob + ((n >> 1) * 16 + (n & 1)) * 1024); });
      wait_vm<0>();
      u32x4 ov[8];
      sfor<8>([&](auto n) { ov[n] = ldsr128(od + (n & 1) * 4096 + (n >> 1) * 1024 + lane * 16); });
      pend = 0;
      sfor<8>([&](auto n) { if (__any(umax4(ov[n]) == SENT)) pend |= 1u << n; });
      if (!pend) break;
    }
    STAMP(10);
  }
  lgk_bar();
  if (r == 0 && g == 0 && w == 0 && lane == 0) __hip_atomic_store((AS1 u64*)(ws + WS_EPOCH + br * 256), tag, RLX_AGENT);
  STAMP(11); CSTAMP(9);
  f32x4 ao[2][2];
  sfor<2>([&](auto b) { ao[b][0] = (f32x4){0, 0, 0, 0}; ao[b][1] = (f32x4){0, 0, 0, 0}; });
  {
    const u32 ob = lds0 + wr * 2048 + lane * 16;
    constexpr int D = 3;
    u32x4 of[4][2];
    auto rdO = [&](auto t) { constexpr int T = t; of[T & 3][0] = ldsr128(ob + T * 4096); of[T & 3][1] = ldsr128(ob + T * 4096 + 1024); };
    sfor<D>([&](auto t) { rdO(t); });
    SB();
    sfor<16>([&](auto t) {
      constexpr int T = t;
      if constexpr (T + D < 16) { rdO(IC(T + D)); SB(); }
      sfor<2>([&](auto b) { sfor<2>([&](auto c) { ao[b][c] = mfma32(mk8(af[T][c][0], af[T][c][1]), asb8(of[T & 3][b]), ao[b][c]); SB(); }); });
    });
  }
  STAMP(12); CSTAMP(10);
  {
    const __amdgpu_buffer_rsrc_t rsU = __builtin_amdgcn_make_buffer_rsrc(outp, 0, 2048 * 1024 * 2, SRD3);
    const u32 op = (r * 128 + 64 * (g & 1) + 32 * wr + l15) * 2048 + br * 1024 + (g >> 1) * 256 + wc * 64 + (G & 1) * 32 + (G >> 1) * 16;
    sfor<2>([&](auto b) {
      u32 a0 = cvtpk(ao[b][0][0], ao[b][0][1]), a1 = cvtpk(ao[b][0][2], ao[b][0][3]);
      u32 b0 = cvtpk(ao[b][1][0], ao[b][1][1]), b1 = cvtpk(ao[b][1][2], ao[b][1][3]);
      auto s0 = __builtin_amdgcn_permlane16_swap(a0, b0, false, false);
      auto s1 = __builtin_amdgcn_permlane16_swap(a1, b1, false, false);
      u32x4 v = {s0[0], s1[0], s0[1], s1[1]};
      __builtin_amdgcn_raw_buffer_store_b128(v, rsU, op + b * 16 * 2048, 0, 16);
    });
  }
  STAMP(13);
}

extern "C" void kernel_launch(void* const* d_in, const int* in_sizes, int n_in, void* d_out, int out_size, void* d_ws, size_t ws_size, hipStream_t stream) {
  (void)in_sizes; (void)n_in; (void)out_size; (void)ws_size;
  hipFuncSetAttribute((const void*)fused_linattn_kernel, hipFuncAttributeMaxDynamicSharedMemorySize, LDS_BYTES);
  fused_linattn_kernel<<<256, 512, LDS_BYTES, stream>>>(d_in[0], d_in[1], d_in[2], d_in[3], d_in[4], d_in[5], d_out, (char*)d_ws
  );
}
```

```cpp
#define LEAKB -1
#include <hip/hip_runtime.h>
#include <stdint.h>
#include <utility>

typedef unsigned u32;
typedef unsigned long long u64;
typedef u32 u32x4 __attribute__((ext_vector_type(4)));
typedef u32 u32x2 __attribute__((ext_vector_type(2)));
typedef u32 u32x8 __attribute__((ext_vector_type(8)));
typedef float f32x4 __attribute__((ext_vector_type(4)));
typedef float f32x2 __attribute__((ext_vector_type(2)));
typedef short s16x4 __attribute__((ext_vector_type(4)));
typedef __bf16 bf16x8 __attribute__((ext_vector_type(8)));
typedef __bf16 bf16x2 __attribute__((ext_vector_type(2)));
#define AS3 __attribute__((address_space(3)))
#define AS1 __attribute__((address_space(1)))
#define AS4 __attribute__((address_space(4)))
#define DI __device__ __forceinline__
#define SB() __builtin_amdgcn_sched_barrier(0)
#define RLX_AGENT __ATOMIC_RELAXED, __HIP_MEMORY_SCOPE_AGENT

constexpr int SRD3 = 0x00020000;
constexpr u32 PG = 16384;
constexpr u32 LDS_BYTES = 163840;
constexpr u32 PX[8] = {1, 3, 5, 7, 9, 2, 6, 0};
constexpr u32 PW[8] = {0, 2, 4, 6, 8, 0, 4, 8};
constexpr u32 QS[8] = {4 * PG, 4 * PG + 8192, 8 * PG, 8 * PG + 8192, 1 * PG, 1 * PG + 8192, 3 * PG, 3 * PG + 8192};
constexpr u32 PO[8] = {5, 7, 8, 9, 2, 1, 6, 0};
constexpr u32 XK = 4 * PG;
constexpr u32 WS_EPOCH = 0;
constexpr u32 WS_F1 = 4096;
constexpr u32 WS_F2 = 4096 + 8192;
constexpr u32 WS_PART = 65536;
constexpr u32 ZSTR = 128;
constexpr u32 PART_BYTES = 2048 + 4 * ZSTR;
constexpr u32 WS_O = 2u << 20;
constexpr u32 WS_USED = 6u << 20;
constexpr u32 SPIN_CAP = 1u << 16;
#define CLR_AT 6
#define OCLR 4
#define PREF 1
#define DEEP 0
#define PF0 0
#define PFD 0
#define PFDS 0
#define PFS 3
#define PF64 0
#define CPF 1
#define PL1 0
#define PL2 0
#define PF1N 64
#define PREF_AT 4
#define CODELEN 20480
#define PREB 0
#define FLATE 2
#define PLATE 0
#define NTX 0
#define NTW 0
#define NTQ 0
#define NTO 0
#define OUTAUX 19
#define PRO 1
#define LDF 1
#define SLO 1
#define XPF 0
#define XPD 0
#define BEARLY 0
#define XE 0
#define MAPO 1
#define OROT 3
#define OMV 0
#define PBS 0
#define OSC 2
#define F2AT 12
#define FL2 0
#define DLY1 8
#define DLY2 8
#define NB 0
#define FE 0
#define OH 0
#define SYT 6
#define ODEP 2
#define PE 0
#define OBS 0
#define LPRI 2
#define LPAT 0
#define NTK 2
#define NTD 3
#define EAF 0
#define EAFC 12
#define GDLY 0
#define PKP 0
#define OSP 1
#define OSD 6
#define OSK 3
#define OPRI 1
#define OAUX 16
#define PAUX 17
#define CAUX 16
#define OHS 0
#define OHB 8
#define FLAG0_AT 14
constexpr u32 SENT = 0xffffffffu;

template <int... Is, class F> DI void sfor_impl(std::integer_sequence<int, Is...>, F&& f) { (f(std::integral_constant<int, Is>{}), ...); }
template <int N, class F> DI void sfor(F&& f) { sfor_impl(std::make_integer_sequence<int, N>{}, f); }

DI u32 rfl(u32 x) { return __builtin_amdgcn_readfirstlane(x); }
DI u32 cvtpk(float lo, float hi) { f32x2 v = {lo, hi}; bf16x2 b = __builtin_convertvector(v, bf16x2); return __builtin_bit_cast(u32, b); }
DI float lo16(u32 p) { return __uint_as_float(p << 16); }
DI float hi16(u32 p) { return __uint_as_float(p & 0xffff0000u); }
DI float phi1(float t) { return __builtin_fmaxf(t + 1.0f, __builtin_amdgcn_fmed3f(__builtin_amdgcn_exp2f(t * 1.44269504088896340736f), 0.f, 1.f)) + 1e-6f; }
DI u32 phi2(float a, float b) { return cvtpk(phi1(a), phi1(b)); }
DI void dma(u32 ldsdst, u32 voff, __amdgpu_buffer_rsrc_t srd, u32 soff) {
  asm volatile("s_mov_b32 m0, %0\n\ts_nop 0\n\tbuffer_load_dwordx4 %1, %2, %3 offen lds" ::"s"(ldsdst), "v"(voff), "s"(srd), "s"(soff) : "m0", "memory");
}
template <int NT> DI void dmat(u32 ldsdst, u32 voff, __amdgpu_buffer_rsrc_t srd, u32 soff) {
  if constexpr (NT == 1) asm volatile("s_mov_b32 m0, %0\n\ts_nop 0\n\tbuffer_load_dwordx4 %1, %2, %3 offen nt lds" ::"s"(ldsdst), "v"(voff), "s"(srd), "s"(soff) : "m0", "memory");
  else if constexpr (NT == 2) asm volatile("s_mov_b32 m0, %0\n\ts_nop 0\n\tbuffer_load_dwordx4 %1, %2, %3 offen sc0 lds" ::"s"(ldsdst), "v"(voff), "s"(srd), "s"(soff) : "m0", "memory");
  else if constexpr (NT == 3) asm volatile("s_mov_b32 m0, %0\n\ts_nop 0\n\tbuffer_load_dwordx4 %1, %2, %3 offen sc1 lds" ::"s"(ldsdst), "v"(voff), "s"(srd), "s"(soff) : "m0", "memory");
  else if constexpr (NT == 4) asm volatile("s_mov_b32 m0, %0\n\ts_nop 0\n\tbuffer_load_dwordx4 %1, %2, %3 offen sc0 nt lds" ::"s"(ldsdst), "v"(voff), "s"(srd), "s"(soff) : "m0", "memory");
  else if constexpr (NT == 5) asm volatile("s_mov_b32 m0, %0\n\ts_nop 0\n\tbuffer_load_dwordx4 %1, %2, %3 offen sc1 nt lds" ::"s"(ldsdst), "v"(voff), "s"(srd), "s"(soff) : "m0", "memory");
  else dma(ldsdst, voff, srd, soff);
}
DI void dma_sc1(u32 ldsdst, u32 voff, __amdgpu_buffer_rsrc_t srd, u32 soff) {
  asm volatile("s_mov_b32 m0, %0\n\ts_nop 0\n\tbuffer_load_dwordx4 %1, %2, %3 offen sc1 lds" ::"s"(ldsdst), "v"(voff), "s"(srd), "s"(soff) : "m0", "memory");
}
DI u32x4 ld_sc1(u32 voff, __amdgpu_buffer_rsrc_t srd, u32 soff) {
  u32x4 v; asm volatile("buffer_load_dwordx4 %0, %1, %2, %3 offen sc1" : "=v"(v) : "v"(voff), "s"(srd), "s"(soff) : "memory"); return v;
}
DI u32x4 ld_reg(u32 voff, __amdgpu_buffer_rsrc_t srd, u32 soff) {
  u32x4 v; asm volatile("buffer_load_dwordx4 %0, %1, %2, %3 offen" : "=v"(v) : "v"(voff), "s"(srd), "s"(soff) : "memory"); return v;
}
DI void ldsw128(u32 a, u32x4 v) { *(AS3 u32x4*)(a) = v; }
DI u32 umax(u32 a, u32 b) { return a > b ? a : b; }
DI u32 umax4(u32x4 v) { return umax(umax(v[0], v[1]), umax(v[2], v[3])); }
DI u32x4 clean(u32x4 v) { return __builtin_elementwise_min(v, (u32x4){SENT - 1, SENT - 1, SENT - 1, SENT - 1}); }
template <int N> DI void wait_vm() { asm volatile("s_waitcnt vmcnt(%0)" ::"n"(N) : "memory"); }
template <int N> DI void wait_vm_bar() { asm volatile("s_waitcnt vmcnt(%0)\n\ts_barrier" ::"n"(N) : "memory"); }
template <int N> DI void wait_lgkm() { __builtin_amdgcn_s_waitcnt(0xC07F | (N << 8)); }
DI u32 opaque(u32 x) { asm volatile("" : "+v"(x)); return x; }
DI void pin(u32x2& a, u32x2& b) { asm volatile("" : "+v"(a), "+v"(b)); }
DI void lgk_bar() { asm volatile("s_waitcnt lgkmcnt(0)\n\ts_barrier" ::: "memory"); }
DI void bar() { asm volatile("s_barrier" ::: "memory"); }
DI u32x4 ldsr128(u32 a) { return *(const AS3 u32x4*)(a); }
DI u32x2 ldsr64(u32 a) { return *(const AS3 u32x2*)(a); }
DI u32x2 ldsr_tr(u32 a) { return __builtin_bit_cast(u32x2, __builtin_amdgcn_ds_read_tr16_b64_v4i16((AS3 s16x4*)(a))); }
DI bf16x8 mk8(u32x2 a, u32x2 b) { u32x4 v = {a[0], a[1], b[0], b[1]}; return __builtin_bit_cast(bf16x8, v); }
DI bf16x8 asb8(u32x4 v) { return __builtin_bit_cast(bf16x8, v); }
DI f32x4 mfma32(bf16x8 a, bf16x8 b, f32x4 c) { return __builtin_amdgcn_mfma_f32_16x16x32_bf16(a, b, c, 0, 0, 0); }
DI f32x4 mfma16(u32x2 a, u32x2 b, f32x4 c) { return __builtin_amdgcn_mfma_f32_16x16x16bf16_1k(__builtin_bit_cast(s16x4, a), __builtin_bit_cast(s16x4, b), c, 0, 0, 0); }

#define STAMP(i) do {} while (0)
#define CSTAMP(i) do {} while (0)
#define CY0(i) do {} while (0)
#define CY1(i, sh) do {} while (0)

#define LSTAMP(i) STAMP(i)
DI void poll_flags(const u64* flag, u64 want) {
  const AS1 u64* p = (const AS1 u64*)flag;
#pragma unroll 1
  for (u32 spins = 0; spins < SPIN_CAP; ++spins) {
    u64 v = __hip_atomic_load(p, RLX_AGENT);
    if (__all(v == want)) break;
    __builtin_amdgcn_s_sleep(1);
  }
  asm volatile("" ::: "memory");
}

using std::integral_constant;
#define IC(n) integral_constant<int, (n)>{}

__global__ __launch_bounds__(512, 2) void fused_linattn_kernel(const void* __restrict__ xA, const void* __restrict__ wA, const void* __restrict__ oA, char* __restrict__ wsA,
                                                               const void* __restrict__ xB, const void* __restrict__ wB, const void* __restrict__ oB, char* __restrict__ wsB,
                                                               void* __restrict__ outq
) {
  extern __shared__ __attribute__((aligned(16))) char sm[];
  u32x8 ka; u64 outa;
  { const AS4 char* kp = (const AS4 char*)__builtin_amdgcn_kernarg_segment_ptr();
    asm volatile("s_load_dwordx8 %0, %2, %3\n\ts_load_dwordx2 %1, %2, 0x40" : "=&s"(ka), "=&s"(outa) : "s"(kp), "s"((blockIdx.x & 1) * 32)); }
  u32 cpa = 0, cpb = 0;
  if constexpr (CPF >= 3) {
    u64 pct; asm volatile("s_getpc_b64 %0" : "=s"(pct));
    if constexpr (CPF == 3 || CPF == 4) { const u32 o1 = (threadIdx.x < PF1N - 1 ? threadIdx.x : PF1N - 1) * 64;
      asm volatile("global_load_dword %0, %1, %2" : "=v"(cpa) : "v"(o1), "s"(pct)); }
    if constexpr (CPF == 3 || CPF == 5) { u32 l2 = ((blockIdx.x * 2) & ~15u) | (threadIdx.x & 15); l2 = l2 < CODELEN / 64 - 1 ? l2 : CODELEN / 64 - 1;
      asm volatile("global_load_dword %0, %1, %2" : "=v"(cpb) : "v"(l2 * 64), "s"(pct)); }
  }
  SB();
  const u32 lds0 = (u32)(uintptr_t)(AS3 char*)sm;
  const int lane = threadIdx.x & 63;
  const u32 w = rfl(threadIdx.x >> 6);
  const u32 cw = w & 3;
  const u32 bid = blockIdx.x, xcd = bid & 7, j = bid >> 3;
  const u32 br = xcd & 1, r = ((xcd >> 1) & 1) * 8 + (j & 7), g = (xcd >> 2) * 4 + (j >> 3);
  const u32 hf = MAPO ? g >> 2 : g & 1, cbo = MAPO ? g & 3 : g >> 1;
#define ROTM 0
  const u32 jl = j & 7, jh = j >> 3;
  const u32 rot = ROTM == 0 ? (jl + 2 * jh) & 7 : ROTM == 1 ? 0 : ROTM == 2 ? 2 * jh : ROTM == 3 ? jl : ROTM == 4 ? ((j & 1) * 4) : ROTM == 5 ? (jh & 1) * 4 : ROTM == 6 ? (j & 1) + 2 * (jh & 1)
                : ROTM == 7 ? (2 * (jl & 3) + 4 * (jh & 1)) & 7 : ROTM == 8 ? (2 * (jl & 3) + 2 * jh) & 7 : ROTM == 9 ? (jl + 4 * (jh & 1)) & 7 : ROTM == 10 ? (2 * (jl & 1) + 4 * (jh & 1)) & 7
                : ROTM == 11 ? ((jl & 3) + 4 * (jh & 1)) & 7 : (jl + 2 * (jh & 1)) & 7;
  constexpr u32 ORD[6][8] = {{0, 1, 2, 3, 4, 5, 6, 7}, {0, 2, 4, 6, 1, 3, 5, 7}, {0, 2, 1, 3, 4, 6, 5, 7}, {0, 4, 2, 6, 1, 5, 3, 7}, {0, 4, 1, 5, 2, 6, 3, 7}, {0, 2, 4, 6, 7, 1, 3, 5}};
  auto slc = [&](int k) { return (rot + ORD[SLO][k]) & 7; };
  const u32 G = lane >> 4, l15 = lane & 15, q4 = (lane >> 2) & 3, p4 = lane & 3;
  const bool loader = LDF ? w < 4 : w >= 4;
  STAMP(0); CY0(98);
  u64 pc0 = 0;
  if constexpr ((CPF == 1 || CPF == 2 || CPF == 4) && PRO != 0) asm volatile("s_getpc_b64 %0" : "=s"(pc0));
#define EWAIT() do { u32 lo_, hi_; asm volatile("s_waitcnt lgkmcnt(0)\n\ts_add_u32 %0, %2, 1\n\ts_addc_u32 %1, %3, 0" : "=&s"(lo_), "=&s"(hi_) : "s"(E[0]), "s"(E[1]) : "scc"); tag = ((u64)hi_ << 32) | lo_; } while (0)
#define SOX0 soX0
#define SOW0 soW0
  u32x2 E;
  u32 voffX = (r * 128 + 8 * cw + (lane >> 3)) * 1024 + (((lane & 7) ^ (4 * (cw & 1) + G)) * 16);
  u32 voffW;
  { u32 f = G | (((cw >> 1) & 1) << 2), cb = (l15 >> 1) ^ f;
    voffW = (4 * cw + G) * 3072 + (512 + g * 64 + (cb & 3) * 16 + (cb >> 2) * 512) * 2 + (lane & 1) * 16; }
  const u32 soW0 = rot * 64 * 3072, soX0 = rot * 128;
  asm volatile("s_waitcnt lgkmcnt(0)" : "+s"(ka), "+s"(outa), "+v"(voffX), "+v"(voffW) : "s"(soW0), "s"(soX0), "s"(lds0 + cw * 1024));
  char* const ws = (char*)(((u64)ka[7] << 32) | ka[6]);
  asm volatile("s_load_dwordx2 %0, %1, %2" : "=&s"(E) : "s"(ws), "s"(WS_EPOCH + br * 256));
  SB();
  void* const outp = (void*)outa;
  const __amdgpu_buffer_rsrc_t rsX = __builtin_amdgcn_make_buffer_rsrc((void*)(((u64)ka[1] << 32) | ka[0]), 0, 2048 * 512 * 2, SRD3);
  const __amdgpu_buffer_rsrc_t rsW = __builtin_amdgcn_make_buffer_rsrc((void*)(((u64)ka[3] << 32) | ka[2]), 0, 512 * 1536 * 2, SRD3);
  const __amdgpu_buffer_rsrc_t rsO = __builtin_amdgcn_make_buffer_rsrc((void*)(((u64)ka[5] << 32) | ka[4]), 0, 512 * 512 * 2, SRD3);
  const __amdgpu_buffer_rsrc_t rsS = __builtin_amdgcn_make_buffer_rsrc((void*)ws, 0, WS_USED, SRD3);

  u64 flo = 0, tag; u32 pfo = 0;
  u32x2 af[16][2][2];
  auto rdAfE = [&](u32 wb, auto t) { sfor<2>([&](auto c) {
      const u32 a = (wb ^ ((2 * cw + c) * 32)) + PO[t >> 1] * PG + (t & 1) * 8192;
      af[t][c][0] = ldsr_tr(a); af[t][c][1] = ldsr_tr(a + 4096); }); };
  auto wbE = [&]() { const u32 rr = 4 * G + q4; return opaque(lds0 + rr * 256 + (rr & 7) * 32 + p4 * 8); };
  if (__builtin_expect(loader, PRO ? 1 : 0)) {
    const u32 ldw = lds0 + cw * 1024;
    auto dmaX = [&](int s) { const u32 so = (PRO && s == 0) ? SOX0 : slc(s) * 128;
      sfor<4>([&](auto i) { dmat<NTX>(ldw + PX[s] * PG + i * 4096, voffX, rsX, so + i * 32768); }); };
    auto dmaW = [&](int s) { const u32 so = (PRO && s == 0) ? SOW0 : slc(s) * 64 * 3072;
      sfor<4>([&](auto i) { dmat<NTW>(ldw + PW[s] * PG + i * 4096, voffW, rsW, so + i * 16 * 3072); }); };
    dmaW(0); dmaX(0);
    STAMP(20);
    if constexpr (PREB >= 1) bar();
    if constexpr (DLY1 < 8) wait_vm<DLY1>();
    dmaW(1); dmaX(1);
    STAMP(21);
    if constexpr (PREB >= 2) bar();
    if constexpr (DLY2 < 16) wait_vm<DLY2>();
    if constexpr (XE == 2) dmaX(4);
    if constexpr (!BEARLY) { dmaW(2); dmaX(2); }
    if constexpr (XE == 1) dmaX(4);
    STAMP(22);
    if constexpr (DEEP) { dmaW(3); dmaX(3); dmaW(4); dmaX(4); }
    EWAIT();
#define CPKEEP asm volatile("" ::"v"(cpa), "v"(cpb))
    STAMP(23);
    u32 voffQ;
    { u32 f = (G & 1) | ((cw & 1) << 1), cb = ((lane & 7) >> 1) ^ f;
      voffQ = (8 * cw + (lane >> 3)) * 3072 + (g * 64 + cb * 16) * 2 + (lane & 1) * 16; }
    u32 voffO;
    { u32 f = 4 * (cw & 1) + G, cb = (l15 >> 1) ^ f;
      voffO = (4 * cw + G) * 1024 + (128 * cbo + cb * 16) * 2 + (lane & 1) * 16; }
    auto dmaQ = [&](int t) { const u32 so = slc(t) * 64 * 3072;
      sfor<2>([&](auto i) { dmat<NTQ>(ldw + QS[t] + i * 4096, voffQ, rsW, so + i * 32 * 3072); }); };
    auto dmaO = [&](int i) { sfor<4>([&](auto u) { dmat<NTO>(ldw + PO[i] * PG + u * 4096, voffO, rsO, (64 * i + 16 * u) * 1024); }); };
    if constexpr (DEEP) {
      wait_vm_bar<32>();
      STAMP(1);
      wait_vm_bar<24>(); dmaW(5);
    } else {
      wait_vm_bar<(BEARLY ? 8 : XE ? 20 : 16)>();
      CPKEEP;
      STAMP(1);
      if constexpr (BEARLY) { dmaW(2); dmaX(2); }
      if constexpr (BEARLY == 2) { wait_vm_bar<8>(); dmaW(3); dmaX(3); dmaW(4); dmaX(4); dmaW(5); } else
      {
      dmaW(3); dmaX(3);
      if constexpr (XE) { wait_vm_bar<20>(); dmaW(4); dmaW(5); }
      else { wait_vm_bar<16>(); dmaW(4); dmaX(4); dmaW(5); }
      }
    }
    wait_vm_bar<(XE == 2 ? 16 : 20)>(); dmaX(5);
    wait_vm_bar<(XE ? 12 : 16)>(); dmaW(6);
    wait_vm_bar<12>(); dmaX(6);
    wait_vm_bar<8>(); dmaW(7);
    wait_vm_bar<4>(); dmaX(7);
    wait_vm_bar<0>(); dmaQ(0); dmaQ(1);
    bar(); dmaQ(2); dmaQ(3);
    u32x4 rq[8], ro[8];
    const u32 lw16 = ldw + lane * 16;
    const u32 fi = br * 512 + (g * 4 + cw) * 16 + l15;
    const u32 fo = br * 512 + ((2 * cw + ((lane >> 1) & 1)) * 4 + hf + 2 * (lane & 1)) * 16 + r;
    u64 fl;
    if constexpr (NB) {
      sfor<8>([&](auto n) { rq[n] = ld_reg(voffQ, rsW, slc(4 + (n >> 1)) * 64 * 3072 + (n & 1) * 32 * 3072); });
      wait_vm_bar<12>();
      wait_vm_bar<8>();
      asm volatile("s_waitcnt vmcnt(4)" : "+v"(rq[0]), "+v"(rq[1]), "+v"(rq[2]), "+v"(rq[3])::"memory");
      sfor<4>([&](auto n) { ldsw128(lw16 + QS[4 + (n >> 1)] + (n & 1) * 4096, rq[n]); });
      lgk_bar();
      asm volatile("s_waitcnt vmcnt(0)" : "+v"(rq[4]), "+v"(rq[5]), "+v"(rq[6]), "+v"(rq[7])::"memory");
      sfor<4>([&](auto n) { ldsw128(lw16 + QS[6 + (n >> 1)] + (n & 1) * 4096, rq[4 + n]); });
      SB();
      asm volatile("buffer_load_dwordx2 %0, %1, %2, 0 offen sc1" : "=v"(fl) : "v"(WS_F1 + fi * 8), "s"(rsS) : "memory");
      sfor<8>([&](auto n) { ro[n] = ld_reg(voffO, rsO, (64 * (6 + (n >> 2)) + 16 * (n & 3)) * 1024); });
      lgk_bar();
      dmaO(0); dmaO(1); dmaO(2);
      asm volatile("s_waitcnt vmcnt(20)\n\ts_barrier" : "+v"(fl)::"memory");
    } else {
    wait_vm_bar<0>();
    if constexpr (PE == 1) asm volatile("buffer_load_dwordx2 %0, %1, %2, 0 offen sc1" : "=v"(fl) : "v"(WS_F1 + fi * 8), "s"(rsS) : "memory");
    bar(); dmaQ(4); dmaQ(5);
    bar();
    }
    const u32 pb = WS_PART + (br * 8 + g) * 16 * PART_BYTES;
    const u32 vk = G * PART_BYTES + cw * 512 + l15 * 16, vz = 2048 + cw * ZSTR + (lane & 3) * 16 + (lane >> 2) * PART_BYTES;
    u32x4 pv[8], zv;
    if constexpr (PE == 2) {
      asm volatile("buffer_load_dwordx2 %0, %1, %2, 0 offen sc1" : "=v"(fl) : "v"(WS_F1 + fi * 8), "s"(rsS) : "memory");
      dmaQ(6); dmaQ(7);
      asm volatile("s_waitcnt vmcnt(4)\n\ts_barrier" : "+v"(fl)::"memory");
      if (!__all(fl == tag)) poll_flags((const u64*)(ws + WS_F1) + fi, tag);
      LSTAMP(4);
      sfor<8>([&](auto p) { pv[p] = ld_sc1(vk, rsS, pb + (p >> 1) * 4 * PART_BYTES + (p & 1) * 256); });
      zv = ld_sc1(vz, rsS, pb);
      dmaO(0); dmaO(1);
      wait_vm_bar<17>();
    } else
    if constexpr (PE) {
      dmaQ(6); dmaQ(7);
      asm volatile("s_waitcnt vmcnt(8)" : "+v"(fl)::"memory");
      if (!__all(fl == tag)) poll_flags((const u64*)(ws + WS_F1) + fi, tag);
      LSTAMP(4);
      sfor<8>([&](auto p) { pv[p] = ld_sc1(vk, rsS, pb + (p >> 1) * 4 * PART_BYTES + (p & 1) * 256); });
      zv = ld_sc1(vz, rsS, pb);
      wait_vm_bar<13>();
      dmaO(0); dmaO(1);
      wait_vm_bar<17>();
    } else
    if constexpr (NB) {
    } else if constexpr (PLATE == 0) {
      asm volatile("buffer_load_dwordx2 %0, %1, %2, 0 offen sc1" : "=v"(fl) : "v"(WS_F1 + fi * 8), "s"(rsS) : "memory");
      dmaQ(6); dmaQ(7);
      wait_vm_bar<5>();
      dmaO(0); dmaO(1);
      asm volatile("s_waitcnt vmcnt(8)\n\ts_barrier" : "+v"(fl)::"memory");
    } else if constexpr (PLATE == 1) {
      dmaQ(6); dmaQ(7);
      asm volatile("buffer_load_dwordx2 %0, %1, %2, 0 offen sc1" : "=v"(fl) : "v"(WS_F1 + fi * 8), "s"(rsS) : "memory");
      wait_vm_bar<5>();
      dmaO(0); dmaO(1);
      wait_vm_bar<9>();
      asm volatile("s_waitcnt vmcnt(8)" : "+v"(fl)::"memory");
    } else {
      dmaQ(6); dmaQ(7);
      wait_vm_bar<4>();
      dmaO(0); dmaO(1);
      wait_vm_bar<8>();
      asm volatile("buffer_load_dwordx2 %0, %1, %2, 0 offen sc1" : "=v"(fl) : "v"(WS_F1 + fi * 8), "s"(rsS) : "memory");
      asm volatile("s_waitcnt vmcnt(0)" : "+v"(fl)::"memory");
    }
    if constexpr (!PE) {
    if (!__all(fl == tag)) poll_flags((const u64*)(ws + WS_F1) + fi, tag);
    LSTAMP(4);
    }
    {
      if constexpr (PE) {
      } else
      if constexpr (PL1) {
        sfor<8>([&](auto p) { pv[p] = ld_reg(vk, rsS, pb + (p >> 1) * 4 * PART_BYTES + (p & 1) * 256); });
        zv = ld_reg(vz, rsS, pb);
      } else {
      sfor<8>([&](auto p) { pv[p] = ld_sc1(vk, rsS, pb + (p >> 1) * 4 * PART_BYTES + (p & 1) * 256); });
      zv = ld_sc1(vz, rsS, pb);
      }
      if constexpr (NB) { dmaO(3); dmaO(4); dmaO(5); } else { dmaO(2); dmaO(3); }
      if constexpr (OMV == 2) { dmaO(4); dmaO(5); }
      if constexpr (OMV == 3) { dmaO(4); dmaO(5); bar(); dmaO(6); dmaO(7);
        if constexpr (FL2 == 1) asm volatile("buffer_load_dwordx2 %0, %1, %2, 0 offen sc1" : "=v"(fl) : "v"(WS_F2 + fo * 8), "s"(rsS) : "memory"); }
      const u32 one = (l15 & 1) ? 0x3f800000u : 0x3f80u, jd = (l15 >> 1) & 3;
      const u32x4 sel = {jd == 0 ? one : 0u, jd == 1 ? one : 0u, jd == 2 ? one : 0u, jd == 3 ? one : 0u};
      const u32x4 zero4 = {0, 0, 0, 0};
      const u32x4 selLo = l15 < 8 ? sel : zero4, selHi = l15 < 8 ? zero4 : sel;
      if constexpr (LPRI) __builtin_amdgcn_s_setprio(LPRI);
      asm volatile("s_waitcnt vmcnt(%9)" : "+v"(pv[0]), "+v"(pv[1]), "+v"(pv[2]), "+v"(pv[3]), "+v"(pv[4]), "+v"(pv[5]), "+v"(pv[6]), "+v"(pv[7]), "+v"(zv) : "n"(PE ? (OMV == 2 ? 24 : 16) : NB ? 12 : OMV == 3 ? 24 + FL2 : OMV == 2 ? 16 : 8));
      if constexpr (NB) asm volatile("" : "+v"(ro[0]), "+v"(ro[1]), "+v"(ro[2]), "+v"(ro[3]), "+v"(ro[4]), "+v"(ro[5]), "+v"(ro[6]), "+v"(ro[7]));
      LSTAMP(5); CY0(93);
      if constexpr (FE == 1) asm volatile("buffer_load_dwordx2 %0, %1, %2, 0 offen sc1" : "=v"(fl) : "v"(WS_F2 + fo * 8), "s"(rsS) : "memory");
      f32x4 s0; float z0, z1, z2, z3;
      auto red = [&](float x) {
        x += __uint_as_float(__builtin_amdgcn_update_dpp(0u, __float_as_uint(x), 0x124, 0xf, 0xf, false));
        x += __uint_as_float(__builtin_amdgcn_update_dpp(0u, __float_as_uint(x), 0x128, 0xf, 0xf, false));
        auto a = __builtin_amdgcn_permlane16_swap(__float_as_uint(x), __float_as_uint(x), false, false);
        x = __uint_as_float(a[0]) + __uint_as_float(a[1]);
        auto c = __builtin_amdgcn_permlane32_swap(__float_as_uint(x), __float_as_uint(x), false, false);
        return __uint_as_float(c[0]) + __uint_as_float(c[1]);
      };
      auto reduce = [&]() {
        s0 = (f32x4){0, 0, 0, 0};
        f32x4 s1 = {0, 0, 0, 0};
        sfor<4>([&](auto p) { s0 = mfma32(asb8(selLo), asb8(pv[2 * p]), s0); s1 = mfma32(asb8(selHi), asb8(pv[2 * p + 1]), s1); });
        s0 += s1;
        f32x4 z = __builtin_bit_cast(f32x4, zv);
        z0 = red(z[0]); z1 = red(z[1]); z2 = red(z[2]); z3 = red(z[3]);
      };
      reduce();
      if (__any(__builtin_isunordered(s0[0], s0[1]) || __builtin_isunordered(s0[2], s0[3]) || __builtin_isunordered(z0, z1) || __builtin_isunordered(z2, z3))) {
#pragma unroll 1
        for (u32 spins = 0; spins < SPIN_CAP; ++spins) {
          u32 pend = 0;
          sfor<8>([&](auto p) { if (__any(umax4(pv[p]) == SENT)) { pend = 1; pv[p] = __builtin_amdgcn_raw_buffer_load_b128(rsS, vk, pb + (p >> 1) * 4 * PART_BYTES + (p & 1) * 256, 16); } });
          if (__any(umax4(zv) == SENT)) { pend = 1; zv = __builtin_amdgcn_raw_buffer_load_b128(rsS, vz, pb, 16); }
          asm volatile("" ::: "memory");
          if (!pend) break;
        }
        reduce();
      }
      const u32 sc = lds0 + XK + cw * 1024;
      *(AS3 u32x2*)(sc + lane * 8) = (u32x2){cvtpk(s0[0], s0[1]), cvtpk(s0[2], s0[3])};
      if (lane < 4) *(AS3 u32x2*)(sc + 512 + lane * 8) = (u32x2){cvtpk(z0, z1), cvtpk(z2, z3)};
    }
    LSTAMP(6); CY1(93, 2);
    if constexpr (FE == 2) asm volatile("buffer_load_dwordx2 %0, %1, %2, 0 offen sc1" : "=v"(fl) : "v"(WS_F2 + fo * 8), "s"(rsS) : "memory");
    if constexpr (OMV == 0 && !NB) { dmaO(4); dmaO(5); }
    STAMP(2);
    if constexpr (EAF == 1) wait_vm<0>(); else if constexpr (EAF == 2) wait_vm<8>();
    lgk_bar();
    if constexpr (OMV == 1) { dmaO(4); dmaO(5); }
    STAMP(3);
    if constexpr (NB) {
      asm volatile("buffer_load_dwordx2 %0, %1, %2, 0 offen sc1" : "=v"(fl) : "v"(WS_F2 + fo * 8), "s"(rsS) : "memory");
      sfor<8>([&](auto n) { ldsw128(lw16 + PO[6 + (n >> 2)] * PG + (n & 3) * 4096, ro[n]); });
    } else if constexpr (OMV == 3) {
      if constexpr (FL2 == 0) asm volatile("buffer_load_dwordx2 %0, %1, %2, 0 offen sc1" : "=v"(fl) : "v"(WS_F2 + fo * 8), "s"(rsS) : "memory");
    } else if constexpr (FLATE == 0) {
      asm volatile("buffer_load_dwordx2 %0, %1, %2, 0 offen sc1" : "=v"(fl) : "v"(WS_F2 + fo * 8), "s"(rsS) : "memory");
      STAMP(8);
      dmaO(6); dmaO(7);
      STAMP(7);
    } else if constexpr (FLATE == 1) {
      dmaO(6); dmaO(7);
      STAMP(8);
      wait_vm<0>();
      STAMP(7);
      asm volatile("buffer_load_dwordx2 %0, %1, %2, 0 offen sc1" : "=v"(fl) : "v"(WS_F2 + fo * 8), "s"(rsS) : "memory");
    } else if constexpr (FLATE == 2) {
      dmaO(6); dmaO(7);
      STAMP(8);
      if constexpr (FE == 0) asm volatile("buffer_load_dwordx2 %0, %1, %2, 0 offen sc1" : "=v"(fl) : "v"(WS_F2 + fo * 8), "s"(rsS) : "memory");
    } else {
      dmaO(6); dmaO(7);
      STAMP(8);
      flo = __hip_atomic_load((const AS1 u64*)(ws + WS_F2) + fo, RLX_AGENT);
      pfo = fo;
    }
    u32 wbl = 0;
    if constexpr (EAF) { SB(); wbl = wbE(); sfor<EAF == 1 ? 12 : 8>([&](auto t) { rdAfE(wbl, t); }); SB(); }
    if constexpr (FLATE < 3) {
      asm volatile("s_waitcnt vmcnt(0)" : "+v"(fl)::"memory");
      if (!__all(fl == tag)) poll_flags((const u64*)(ws + WS_F2) + fo, tag);
    } else {
      wait_vm<1>();
    }
    if constexpr (NB) lgk_bar(); else
    bar();
    if constexpr (EAF) { constexpr int F = EAF == 1 ? 12 : 8; SB(); sfor<16 - F>([&](auto t) { rdAfE(wbl, IC(F + t)); }); SB(); }
  } else {
    u32 cpf = 0, cpf2 = 0;
    if constexpr (CPF == 1 || CPF == 2 || CPF == 4) {
      u64 pc; const u32 len = CODELEN;
      if constexpr (PRO) pc = pc0; else asm volatile("s_getpc_b64 %0" : "=s"(pc));
      pc &= ~63ull;
      if constexpr (CPF == 1 || CPF == 4) {
        const u32 off = (j * 16 + (lane & 15)) * 64;
        if (off < len && cw == 0 && lane < 16) cpf = *(const AS1 u32*)(pc + off);
      } else {
        const u32 off = (cw * 64 + lane) * 64;
        cpf = *(const AS1 u32*)(pc + off);
        cpf2 = *(const AS1 u32*)(pc + (off + 16384 < len ? off + 16384 : off));
      }
    }
    __builtin_amdgcn_s_setprio(1);
    const u32 xa0 = lds0 + l15 * 128 + ((G ^ (l15 >> 1)) * 16), xa1 = xa0 ^ 64;
    const u32 wk = lds0 + (8 * G + q4) * 256 + ((cw ^ (q4 | ((G & 1) << 2))) * 32) + p4 * 8;
    const u32 wv = wk ^ 128;
    const u32 xa0w[3] = {xa0, opaque(xa0 + 65536), opaque(xa0 + 131072)}, xa1w[3] = {xa1, opaque(xa1 + 65536), opaque(xa1 + 131072)};
    const u32 wkw[3] = {wk, opaque(wk + 65536), opaque(wk + 131072)}, wvw[3] = {wv, opaque(wv + 65536), opaque(wv + 131072)};
    f32x4 ak[8], av[8];
    sfor<8>([&](auto b) { ak[b] = (f32x4){0, 0, 0, 0}; av[b] = (f32x4){0, 0, 0, 0}; });
    u32x4 xf[2][8];
    u32x2 kf[2][2], vf[2][2];
    auto rdW = [&](auto i, auto c) { constexpr int I = i, C = c, Bf = I & 1; constexpr u32 o = PW[(I >> 1) & 7] * PG + (I & 1) * 8192 + (C & 1) * 1024;
      if constexpr (C < 2) kf[Bf][C] = ldsr_tr(wkw[o >> 16] + (o & 0xffff)); else vf[Bf][C & 1] = ldsr_tr(wvw[o >> 16] + (o & 0xffff)); };
    auto rdX = [&](auto i, auto b) { constexpr int I = i, Bf = I & 1; constexpr u32 o = PX[(I >> 1) & 7] * PG + b * 2048;
      xf[Bf][b] = ldsr128((I & 1 ? xa1w : xa0w)[o >> 16] + (o & 0xffff)); };
    const u32 rh = cw & 1, hp = cw >> 1;
    const u32 pbase = WS_PART + ((br * 8 + g) * 16 + r) * PART_BYTES;
    const u32 vpk = (G & 1) ? 0x7fffffc0u : pbase + cw * 512 + (G >> 1) * 256 + l15 * 16, vpz = l15 ? 0x7fffffc0u : pbase + 2048 + cw * ZSTR + G * 16;
    const u32 vo = lane * 16 + WS_O + ((br * 128 + r * 8 + 4 * rh) * 16 + g * 2 + hp) * 1024;
    const u32x4 sent = {SENT, SENT, SENT, SENT};
    u32 pf = 0;
    u32 pg[6] = {0, 0, 0, 0, 0, 0};
    if constexpr (PF0 == 3) {
      const u32 v = j * 4 + cw, s = PFS + v / 24, u = v % 24;
      const u32 n = PFD + (s - PFS) * PFDS;
#pragma unroll 1
      for (u32 i = 0; i < n; ++i) __builtin_amdgcn_s_sleep(1);
      const u32 sl = (s + rot) & 7;
      if (s < 8) {
        if (u < 16) pg[0] = __builtin_amdgcn_raw_buffer_load_b32(rsX, (((r & 8) + (u >> 1)) * 128 + (u & 1) * 64 + lane) * 1024 + sl * 128, 0, 0);
        else pg[1] = __builtin_amdgcn_raw_buffer_load_b32(rsW, (sl * 64 + lane) * 3072 + 1024 + (u & 1) * 1024 + ((g & 4) + ((u - 16) >> 1)) * 128, 0, 0);
      }
    } else if constexpr (PF0) {
      if constexpr (PFD) __builtin_amdgcn_s_sleep(PFD);
      const u32 OOB = 0x7fffff00u;
      const u32 sx = (j >> 3) * 4 + cw, sw = (j & 7) * 4 + cw;
      const u32 ax = (PF0 >= 2 && sx < 6) ? (r * 128 + (sx & 1) * 64 + lane) * 1024 + (((5 + (sx >> 1) + rot) & 7)) * 128 : OOB;
      const u32 aw = sw < 6 ? (PF0 >= 2 ? ((((5 + (sw >> 1) + rot) & 7)) * 64 + lane) * 3072 + (512 + (sw & 1) * 512 + g * 64) * 2 : OOB)
                   : sw < 14 ? ((sw - 6) * 64 + lane) * 3072 + g * 128 : OOB;
      const u32 ao = (sw >= 16 && sw < 24) ? (((g & 1) * 8 + sw - 16) * 32 + (lane >> 1)) * 1024 + (g >> 1) * 256 + (lane & 1) * 128 : OOB;
      pg[0] = __builtin_amdgcn_raw_buffer_load_b32(rsX, ax, 0, 0);
      pg[1] = __builtin_amdgcn_raw_buffer_load_b32(rsW, aw, 0, 0);
      pg[2] = __builtin_amdgcn_raw_buffer_load_b32(rsO, ao, 0, 0);
      if constexpr (PF64) {
        pg[3] = __builtin_amdgcn_raw_buffer_load_b32(rsX, ax + 64, 0, 0);
        pg[4] = __builtin_amdgcn_raw_buffer_load_b32(rsW, aw + 64, 0, 0);
        pg[5] = __builtin_amdgcn_raw_buffer_load_b32(rsO, ao + 64, 0, 0);
      }
    }
    auto xpf = [&]() { if (cw >= 2) pg[0] = __builtin_amdgcn_raw_buffer_load_b32(rsX, (r * 128 + (cw & 1) * 64 + lane) * 1024 + slc(4) * 128, 0, 0); };
    if constexpr (XPF == 2) {
#pragma unroll 1
      for (u32 i = 0; i < XPD; ++i) __builtin_amdgcn_s_sleep(1);
      xpf();
    }
    EWAIT();
    if constexpr (PREB >= 1) bar();
    if constexpr (PREB >= 2) bar();
    bar();
    STAMP(1); CSTAMP(1); CY0(92);
    if constexpr (XPF == 1) xpf();
    sfor<4>([&](auto c) { rdW(IC(0), c); });
    sfor<8>([&](auto b) { rdX(IC(0), b); });
    sfor<16>([&](auto i) {
      constexpr int I = i, Bf = I & 1;
      constexpr bool RD = I < 15;
      if constexpr (I & 1) { wait_lgkm<0>(); bar(); STAMP(40 + I); }
      else wait_lgkm<4>();
      if constexpr (I == PREF_AT && PREF) {
        const u32 h64 = PF64 ? (cw >> 1) * 64 : 0;
        if (PF64 ? (cw & 1) == 0 : cw == 0) pf = __builtin_amdgcn_raw_buffer_load_b32(rsW, ((j & 7) * 64 + lane) * 3072 + g * 128 + h64, 0, 0);
        if constexpr (MAPO) { if (cw == 1 || cw == 2) pf = __builtin_amdgcn_raw_buffer_load_b32(rsO, (j * 128 + (cw - 1) * 64 + lane) * 128, 0, 0); } else
        if (PF64 ? (cw & 1) == 1 : cw == 1) pf = __builtin_amdgcn_raw_buffer_load_b32(rsO, (((g & 1) * 8 + (j & 7)) * 32 + (lane >> 1)) * 1024 + (g >> 1) * 256 + (lane & 1) * 128 + h64, 0, 0);
      }
      if constexpr (I == FLAG0_AT) {
        wait_vm<0>();
        asm volatile("" ::"v"(cpf), "v"(cpf2), "v"(cpa), "v"(cpb));
        asm volatile("" ::"v"(pf), "v"(pg[0]), "v"(pg[1]), "v"(pg[2]), "v"(pg[3]), "v"(pg[4]), "v"(pg[5]));
        if (lane == 0) __hip_atomic_store((AS1 u64*)(ws + WS_F1) + ((br * 8 + g) * 4 + cw) * 16 + r, tag, RLX_AGENT);
      }
      SB();
      const bf16x8 kb = mk8(kf[Bf][0], kf[Bf][1]), vb = mk8(vf[Bf][0], vf[Bf][1]);
      sfor<16>([&](auto m) {
        constexpr int M = m, Bk = M >> 1;
        if constexpr (M == 8 && !(I & 1)) { wait_lgkm<(RD ? 8 : 0)>(); SB(); }
        if constexpr (M & 1) av[Bk] = mfma32(asb8(xf[Bf][Bk]), vb, av[Bk]); else ak[Bk] = mfma32(asb8(xf[Bf][Bk]), kb, ak[Bk]);
        SB();
        if constexpr (RD && M < 4) { rdW(IC(I + 1), m); SB(); }
        if constexpr (RD && M >= 4 && M < 12) { rdX(IC(I + 1), IC(M - 4)); SB(); }
        if constexpr (M == 13 && I >= CLR_AT && I < CLR_AT + 2) {
          __builtin_amdgcn_raw_buffer_store_b128(sent, rsS, I == CLR_AT ? vpk : vpz, 0, 16);
          SB();
        }
      });
    });
    STAMP(2); CSTAMP(2); CY1(92, 4);
    {
      const u32x4 ones = {0x3f803f80u, 0x3f803f80u, 0x3f803f80u, 0x3f803f80u};
      f32x4 kvp = {0, 0, 0, 0}, zp = {0, 0, 0, 0};
      sfor<4>([&](auto h) {
        u32x4 pk = {phi2(ak[2 * h][0], ak[2 * h][1]), phi2(ak[2 * h][2], ak[2 * h][3]), phi2(ak[2 * h + 1][0], ak[2 * h + 1][1]), phi2(ak[2 * h + 1][2], ak[2 * h + 1][3])};
        u32x4 vv = {cvtpk(av[2 * h][0], av[2 * h][1]), cvtpk(av[2 * h][2], av[2 * h][3]), cvtpk(av[2 * h + 1][0], av[2 * h + 1][1]), cvtpk(av[2 * h + 1][2], av[2 * h + 1][3])};
        kvp = mfma32(asb8(pk), asb8(vv), kvp);
        zp = mfma32(asb8(pk), asb8(ones), zp);
        if constexpr (OCLR == 1) { __builtin_amdgcn_raw_buffer_store_b128(sent, rsS, vo + h * 16384, 0, 16); SB(); }
      });
      const u32 w0 = cvtpk(kvp[0], kvp[1]), w1 = cvtpk(kvp[2], kvp[3]);
      const auto s0 = __builtin_amdgcn_permlane16_swap(w0, w0, false, false), s1 = __builtin_amdgcn_permlane16_swap(w1, w1, false, false);
      __builtin_amdgcn_raw_buffer_store_b128(clean((u32x4){s0[0], s1[0], s0[1], s1[1]}), rsS, vpk, 0, PAUX);
      __builtin_amdgcn_raw_buffer_store_b128(clean(__builtin_bit_cast(u32x4, zp)), rsS, vpz, 0, PAUX);
      if constexpr (OCLR == 3) sfor<4>([&](auto b) { __builtin_amdgcn_raw_buffer_store_b128(sent, rsS, vo + b * 16384, 0, 16); });
    }
    STAMP(3);
    const u32 xb0 = xa0 + rh * 8192, xb1 = xb0 ^ 64;
    const u32 wq0 = lds0 + (8 * G + q4) * 128 + (((2 * hp) ^ (((q4 >> 1) & 1) | ((G & 1) << 1))) * 32) + p4 * 8, wq1 = wq0 ^ 32;
    const u32 xb0w[3] = {opaque(xb0), opaque(xb0 + 65536), opaque(xb0 + 131072)}, xb1w[3] = {opaque(xb1), opaque(xb1 + 65536), opaque(xb1 + 131072)};
    const u32 wqw[3][2] = {{opaque(wq0), opaque(wq1)}, {opaque(wq0 + 65536), opaque(wq1 + 65536)}, {opaque(wq0 + 131072), opaque(wq1 + 131072)}};
    f32x4 aq[4][2];
    sfor<4>([&](auto b) { aq[b][0] = (f32x4){0, 0, 0, 0}; aq[b][1] = (f32x4){0, 0, 0, 0}; });
    constexpr int NBUF = NB ? 3 : 2;
    u32x4 xq[NBUF][4];
    u32x2 qf[NBUF][2][2];
    auto rdQ = [&](auto i, auto h, auto c) { constexpr int I = i, Bf = I % NBUF; constexpr u32 o = QS[(I >> 1) & 7] + (I & 1) * 4096 + c * 512;
      qf[Bf][h][c] = ldsr_tr(wqw[o >> 16][h] + (o & 0xffff)); };
    auto rdY = [&](auto i, auto b) { constexpr int I = i, Bf = I % NBUF; constexpr u32 o = PX[(I >> 1) & 7] * PG + b * 2048;
      xq[Bf][b] = ldsr128((I & 1 ? xb1w : xb0w)[o >> 16] + (o & 0xffff)); };
    auto rdB = [&](auto i, auto n) { constexpr int N = n; if constexpr (N < 4) rdQ(i, IC(N >> 1), IC(N & 1)); else rdY(i, IC(N - 4)); };
    bar();
    STAMP(59); CY0(96);
    sfor<8>([&](auto n) { rdB(IC(0), n); });
    if constexpr (NB) {
      SB();
      sfor<8>([&](auto n) { rdB(IC(1), n); });
      SB();
      sfor<16>([&](auto i) {
        constexpr int I = i, Bf = I % 3;
        constexpr bool RD = I < 14;
        wait_lgkm<(I < 15 ? 8 : 0)>();
        if constexpr (I == 2 || I == 4 || I == 8 || I == 12) { bar(); STAMP(60 + I); }
        if constexpr (I == F2AT) {
          wait_vm<0>();
          if (lane == 0) __hip_atomic_store((AS1 u64*)(ws + WS_F2) + ((br * 8 + g) * 4 + cw) * 16 + r, tag, RLX_AGENT);
        }
        SB();
        const bf16x8 q0 = mk8(qf[Bf][0][0], qf[Bf][0][1]), q1 = mk8(qf[Bf][1][0], qf[Bf][1][1]);
        sfor<8>([&](auto m) {
          constexpr int M = m, Bk = M >> 1;
          aq[Bk][M & 1] = mfma32((M & 1) ? q1 : q0, asb8(xq[Bf][Bk]), aq[Bk][M & 1]); SB();
          if constexpr (RD) { rdB(IC(I + 2), m); SB(); }
          if constexpr (OCLR == 4 && M == 7 && !(I & 1) && I < 8) { __builtin_amdgcn_raw_buffer_store_b128(sent, rsS, vo + (I >> 1) * 16384, 0, CAUX); SB(); }
        });
      });
    } else
    sfor<16>([&](auto i) {
      constexpr int I = i, Bf = I & 1;
      constexpr bool RD = I < 15;
      if constexpr (I == 1 || I == 3 || I == 7 || I == 11) { wait_lgkm<0>(); bar(); STAMP(60 + I); }
      else wait_lgkm<(PBS == 1 ? 0 : 2)>();
      if constexpr (I == F2AT) {
        wait_vm<0>();
        if (lane == 0) __hip_atomic_store((AS1 u64*)(ws + WS_F2) + ((br * 8 + g) * 4 + cw) * 16 + r, tag, RLX_AGENT);
      }
      SB();
      const bf16x8 q0 = mk8(qf[Bf][0][0], qf[Bf][0][1]), q1 = mk8(qf[Bf][1][0], qf[Bf][1][1]);
      sfor<8>([&](auto m) {
        constexpr int M = m, Bk = M >> 1;
        if constexpr (PBS == 0 && M == 4 && !(I == 1 || I == 3 || I == 7 || I == 11)) { wait_lgkm<(RD ? 4 : 0)>(); if constexpr (OMV == 3 && I == 15) bar(); SB(); }
        aq[Bk][M & 1] = mfma32((M & 1) ? q1 : q0, asb8(xq[Bf][Bk]), aq[Bk][M & 1]); SB();
        if constexpr (RD && PBS == 0) { rdB(IC(I + 1), m); SB(); }
        if constexpr (RD && PBS == 1 && M < 4) { rdB(IC(I + 1), IC(2 * M)); rdB(IC(I + 1), IC(2 * M + 1)); SB(); }
        if constexpr (OCLR == 2 && M == 7 && (I & 3) == 1) { __builtin_amdgcn_raw_buffer_store_b128(sent, rsS, vo + (I >> 2) * 16384, 0, 16); SB(); }
        if constexpr (OCLR == 4 && M == 7 && !(I & 1) && I < 8) { __builtin_amdgcn_raw_buffer_store_b128(sent, rsS, vo + (I >> 1) * 16384, 0, CAUX); SB(); }
      });
    });
    STAMP(5); CY1(96, 3); CY0(95);
    if constexpr (OCLR != 2 && OCLR != 4 && F2AT < 0) {
      wait_vm<0>();
      if (lane == 0) __hip_atomic_store((AS1 u64*)(ws + WS_F2) + ((br * 8 + g) * 4 + cw) * 16 + r, tag, RLX_AGENT);
    }
    u32x2 pq[4][2];
    sfor<4>([&](auto b) { sfor<2>([&](auto h) { pq[b][h] = (u32x2){phi2(aq[b][h][0], aq[b][h][1]), phi2(aq[b][h][2], aq[b][h][3])}; }); });
    sfor<4>([&](auto b) { pin(pq[b][0], pq[b][1]); });
    if constexpr ((OCLR == 2 || OCLR == 4) && F2AT < 0) {
      wait_vm<0>();
      if (lane == 0) __hip_atomic_store((AS1 u64*)(ws + WS_F2) + ((br * 8 + g) * 4 + cw) * 16 + r, tag, RLX_AGENT);
    }
    STAMP(6); CY1(95, 2);
    u32 akv = 0, azz = 0, evm = 0;
    if constexpr (OSC != 0) { akv = opaque(lds0 + XK + 2 * hp * 1024 + lane * 8); azz = opaque(lds0 + XK + 2 * hp * 1024 + 512 + G * 8); evm = opaque((l15 & 1) ? 0u : ~0u); SB(); }
    bar();
    STAMP(7); CY0(94);
    if constexpr (OSC == 0) {
    sfor<2>([&](auto h) {
      const u32 sc = lds0 + XK + (2 * hp + h) * 1024;
      const u32x2 kvA = ldsr64(sc + lane * 8), zA = ldsr64(sc + 512 + G * 8);
      sfor<4>([&](auto b) {
        f32x4 num = mfma16(kvA, pq[b][h], (f32x4){0, 0, 0, 0});
        f32x4 den = mfma16(zA, pq[b][h], (f32x4){0, 0, 0, 0});
        float rd = __builtin_amdgcn_rcpf(den[0] + 1e-6f);
        pq[b][h] = (u32x2){cvtpk(num[0] * rd, num[1] * rd), cvtpk(num[2] * rd, num[3] * rd)};
      });
    });
    sfor<4>([&](auto b) {
      u32x4 v = {pq[b][0][0], pq[b][0][1], pq[b][1][0], pq[b][1][1]};
      __builtin_amdgcn_raw_buffer_store_b128(clean(v), rsS, vo + b * 16384, 0, 16);
    });
    } else {
      u32x2 kvA[2], zA[2];
      zA[0] = ldsr64(azz); zA[1] = ldsr64(azz + 1024);
      kvA[0] = ldsr64(akv); kvA[1] = ldsr64(akv + 1024);
      SB();
      f32x4 den[4][2], num[4][2];
      float rd[4][2];
      if constexpr (OSC == 1) {
        sfor<4>([&](auto b) { sfor<2>([&](auto h) { den[b][h] = mfma16(zA[h], pq[b][h], (f32x4){0, 0, 0, 0}); SB(); }); });
      } else {
        const u32 ev = evm;
        const u32x4 zz = {zA[0][0] & ev, zA[0][1] & ev, zA[1][0] & ~ev, zA[1][1] & ~ev};
        SB();
        sfor<4>([&](auto b) { u32x4 v = {pq[b][0][0], pq[b][0][1], pq[b][1][0], pq[b][1][1]};
          f32x4 d = mfma32(asb8(zz), asb8(v), (f32x4){0, 0, 0, 0}); den[b][0][0] = d[0]; den[b][1][0] = d[1]; SB(); });
      }
      auto rcp = [&](auto b) { sfor<2>([&](auto h) { rd[b][h] = __builtin_amdgcn_rcpf(den[b][h][0] + 1e-6f); }); SB(); };
      auto mmn = [&](auto b, auto h) { num[b][h] = mfma16(kvA[h], pq[b][h], (f32x4){0, 0, 0, 0}); SB(); };
      auto fin = [&](auto b, auto h) { const f32x4 t = num[b][h] * rd[b][h]; pq[b][h] = (u32x2){cvtpk(t[0], t[1]), cvtpk(t[2], t[3])}; SB(); };
      auto sto = [&](auto b) { u32x4 v = {pq[b][0][0], pq[b][0][1], pq[b][1][0], pq[b][1][1]};
        __builtin_amdgcn_raw_buffer_store_b128(clean(v), rsS, vo + b * 16384, 0, OAUX); SB(); };
      mmn(IC(0), IC(0)); rcp(IC(0));
      mmn(IC(0), IC(1)); rcp(IC(1));
      mmn(IC(1), IC(0)); fin(IC(0), IC(0));
      mmn(IC(1), IC(1)); fin(IC(0), IC(1)); rcp(IC(2));
      mmn(IC(2), IC(0)); sto(IC(0)); fin(IC(1), IC(0));
      mmn(IC(2), IC(1)); fin(IC(1), IC(1)); rcp(IC(3));
      mmn(IC(3), IC(0)); sto(IC(1)); fin(IC(2), IC(0));
      mmn(IC(3), IC(1)); fin(IC(2), IC(1)); sto(IC(2));
      fin(IC(3), IC(0)); fin(IC(3), IC(1)); sto(IC(3));
    }
    STAMP(8); CY1(94, 2);
    if constexpr (EAF) {
      SB(); const u32 wbc = wbE();
      sfor<EAFC>([&](auto t) { rdAfE(wbc, t); }); SB();
      bar();
      SB(); sfor<16 - EAFC>([&](auto t) { rdAfE(wbc, IC(EAFC + t)); }); SB();
    } else
    lgk_bar();
  }
  const u32 wr = w >> 2, wc = w & 3;
  if constexpr (!EAF) {
    const u32 rr = 4 * G + q4;
    const u32 wb = lds0 + rr * 256 + (rr & 7) * 32 + p4 * 8;
    sfor<16>([&](auto t) { sfor<2>([&](auto c) {
      const u32 a = (wb ^ ((2 * wc + c) * 32)) + PO[t >> 1] * PG + (t & 1) * 8192;
      af[t][c][0] = ldsr_tr(a); af[t][c][1] = ldsr_tr(a + 4096); }); });
  }
  STAMP(9);
  __builtin_amdgcn_s_setprio(0);
  lgk_bar();
  if constexpr (GDLY > 0) __builtin_amdgcn_s_sleep(GDLY);
  const u32 obw = WS_O + (br * 128 + r * 8 + 4 * hf) * 16 * 1024;
  {
    const u32 ox = cbo & 3;
    if constexpr (OHS) {
      sfor<8>([&](auto n) { const u32 rb = ((n >> 2) * 2 + (n & 1)) ^ ox, ks = w + 8 * ((n >> 1) & 1);
        dma_sc1(lds0 + ks * 4096 + rb * 1024, lane * 16, rsS, obw + (rb * 16 + ks) * 1024); });
    } else
    sfor<8>([&](auto n) { dma_sc1(lds0 + (w + 8 * (n >> 2)) * 4096 + (((n & 3) ^ ox) * 1024), lane * 16, rsS, obw + (((n & 3) ^ ox) * 16 + w + 8 * (n >> 2)) * 1024); });
    if constexpr (NTK == 2 && !OHS) wait_vm_bar<0>(); else wait_vm_bar<4>();
  }
  STAMP(11); CSTAMP(9); CY0(97);
  f32x4 ao[2][2];
  sfor<2>([&](auto b) { ao[b][0] = (f32x4){0, 0, 0, 0}; ao[b][1] = (f32x4){0, 0, 0, 0}; });
  bool early = false;
  const u32 op2 = (r * 128 + 64 * hf + 32 * wr + l15) * 2048 + br * 1024 + cbo * 256 + wc * 64 + (G & 1) * 32 + (G >> 1) * 16;
  auto stU = [&](auto b) {
    const u64 oq = (u64)(uintptr_t)outp;
    const __amdgpu_buffer_rsrc_t rsU2 = __builtin_amdgcn_make_buffer_rsrc((void*)(((u64)rfl((u32)(oq >> 32)) << 32) | rfl((u32)oq)), 0, 2048 * 1024 * 2, SRD3);
    u32 a0 = cvtpk(ao[b][0][0], ao[b][0][1]), a1 = cvtpk(ao[b][0][2], ao[b][0][3]);
    u32 b0 = cvtpk(ao[b][1][0], ao[b][1][1]), b1 = cvtpk(ao[b][1][2], ao[b][1][3]);
    auto s0 = __builtin_amdgcn_permlane16_swap(a0, b0, false, false);
    auto s1 = __builtin_amdgcn_permlane16_swap(a1, b1, false, false);
    u32x4 v = {s0[0], s1[0], s0[1], s1[1]};
    __builtin_amdgcn_raw_buffer_store_b128(v, rsU2, op2 + b * 16 * 2048, 0, OUTAUX);
  };
  {
    const u32 ob = lds0 + wr * 2048 + lane * 16;
    const bool first = wr == ((cbo >> 1) & 1);
    if constexpr (OHS) { if (first) __builtin_amdgcn_s_setprio(2); else wait_vm_bar<0>(); }
    if constexpr (OPRI == 3) { if (first) __builtin_amdgcn_s_setprio(2); }
    if constexpr (OPRI == 1) { if (wr == 0) __builtin_amdgcn_s_setprio(2); }
    if constexpr (OPRI == 2) { if (wr == 1) __builtin_amdgcn_s_setprio(2); }
    if constexpr (OSP) {
      u32x4 og[8];
      auto rdP = [&](auto n) { constexpr int N = n; og[N & 7] = ldsr128(ob + (N & 15) * 4096 + (N >> 4) * 1024); };
      sfor<OSD>([&](auto n) { rdP(n); });
      SB();
      sfor<32>([&](auto n) {
        constexpr int N = n, T = N & 15, B = N >> 4;
        if constexpr (OHS && N == OHB) { if (first) wait_vm_bar<0>(); SB(); }
        if constexpr (N + OSD < 32) { rdP(IC(N + OSD)); SB(); }
        sfor<2>([&](auto c) { ao[B][c] = mfma32(mk8(af[T][c][0], af[T][c][1]), asb8(og[N & 7]), ao[B][c]); SB(); });
        if constexpr (N == 15 + OSK) {
          const bool n0 = __builtin_isunordered(ao[0][0][0], ao[0][0][1]) | __builtin_isunordered(ao[0][0][2], ao[0][0][3]) | __builtin_isunordered(ao[0][1][0], ao[0][1][1]) | __builtin_isunordered(ao[0][1][2], ao[0][1][3]);
          early = !__any(n0);
          if (early) stU(IC(0));
          SB();
        }
      });
    } else {
    constexpr int D = NTD;
    u32x4 of[4][2];
    auto rdO = [&](auto t) { constexpr int T = t; of[T & 3][0] = ldsr128(ob + T * 4096); of[T & 3][1] = ldsr128(ob + T * 4096 + 1024); };
    sfor<D>([&](auto t) { rdO(t); });
    SB();
    sfor<16>([&](auto t) {
      constexpr int T = t;
      if constexpr (NTK == 1 && T + D == 8) { wait_vm_bar<0>(); SB(); }
      if constexpr (T + D < 16) { rdO(IC(T + D)); SB(); }
      sfor<2>([&](auto b) { sfor<2>([&](auto c) { ao[b][c] = mfma32(mk8(af[T][c][0], af[T][c][1]), asb8(of[T & 3][b]), ao[b][c]); SB(); }); });
    });
    }
    CY1(97, 2);
    bool nan = false;
    sfor<2>([&](auto b) { sfor<2>([&](auto c) { nan |= __builtin_isunordered(ao[b][c][0], ao[b][c][1]) | __builtin_isunordered(ao[b][c][2], ao[b][c][3]); }); });
    if (__builtin_expect(__any(nan), 0)) {
      early = false;
      sfor<2>([&](auto b) { ao[b][0] = (f32x4){0, 0, 0, 0}; ao[b][1] = (f32x4){0, 0, 0, 0}; });
      sfor<4>([&](auto q) {
        u32x4 v[8];
        sfor<8>([&](auto n) { v[n] = ldsr128(ob + (4 * q + (n >> 1)) * 4096 + (n & 1) * 1024); });
#pragma unroll 1
        for (u32 spins = 0; spins < SPIN_CAP; ++spins) {
          u32 pend = 0;
          sfor<8>([&](auto n) { if (__any(umax4(v[n]) == SENT)) { pend = 1;
            v[n] = __builtin_amdgcn_raw_buffer_load_b128(rsS, lane * 16, obw + ((2 * wr + (n & 1)) * 16 + 4 * q + (n >> 1)) * 1024, 16); } });
          asm volatile("" ::: "memory");
          if (!pend) break;
        }
        sfor<8>([&](auto n) { sfor<2>([&](auto c) { constexpr int T = 4 * q + (n >> 1); ao[n & 1][c] = mfma32(mk8(af[T][c][0], af[T][c][1]), asb8(v[n]), ao[n & 1][c]); }); });
      });
    }
  }
  if (r == 0 && g == 0 && w == 0 && lane == 0) __hip_atomic_store((AS1 u64*)(ws + WS_EPOCH + br * 256), tag, RLX_AGENT);
  STAMP(12); CSTAMP(10);
  if (!early) stU(IC(0));
  stU(IC(1));
  STAMP(13);
}

extern "C" void kernel_launch(void* const* d_in, const int* in_sizes, int n_in, void* d_out, int out_size, void* d_ws, size_t ws_size, hipStream_t stream) {
  (void)in_sizes; (void)n_in; (void)out_size; (void)ws_size;
  hipFuncSetAttribute((const void*)fused_linattn_kernel, hipFuncAttributeMaxDynamicSharedMemorySize, LDS_BYTES);
  fused_linattn_kernel<<<256, 512, LDS_BYTES, stream>>>(d_in[0], d_in[1], d_in[2], (char*)d_ws, d_in[3], d_in[4], d_in[5], (char*)d_ws, d_out
  );
}
```
